```python
import torch

def _softmax(x, axis=-1):
    return torch.softmax(x, dim=axis)
def _gelu(x):
    return torch.nn.functional.gelu(x, approximate="tanh")
def _silu(x):
    return torch.nn.functional.silu(x)
def _sigmoid(x):
    return torch.sigmoid(x)
def _layernorm(x, axis=-1, eps=1e-5):
    m = x.mean(dim=axis, keepdim=True)
    v = x.var(dim=axis, keepdim=True, unbiased=False)
    return (x - m) / torch.sqrt(v + eps)
def _scatter_add(tgt, idx, src, axis=0):
    return tgt.clone().index_add_(axis, idx, src)
def _segment_sum(src, seg, n):
    out = torch.zeros((n,) + src.shape[1:], dtype=src.dtype)
    return out.index_add_(0, seg, src)
def _pad(x, pw):
    flat = tuple(v for pair in reversed(pw) for v in pair)
    return torch.nn.functional.pad(x, flat)

kernel_name = 'standalone__self_sup_conv_stem_wavlm__speech'

def setup_inputs():

    _w = (lambda a: (lambda t: t.to(torch.bfloat16) if t.is_floating_point() else t)(torch.as_tensor(a)))
    B, T, D, K = 2, 16000, 256, 31
    scale = (K * D) ** -0.5
    return {
        "x": _w((lambda *s: torch.randn(*s, dtype=torch.bfloat16) * 0.05)(B, T, D)),
        "w": _w((lambda *s: torch.randn(*s, dtype=torch.bfloat16) * 0.05)(K, D, D) * scale),
        "gn_gamma": _w((lambda *s: torch.randn(*s, dtype=torch.bfloat16) * 0.05)(D) * 0.1 + 1.0),
        "gn_beta": _w((lambda *s: torch.randn(*s, dtype=torch.bfloat16) * 0.05)(D) * 0.1),
    }

def reference(x, w, gn_gamma, gn_beta):
    B, T, D = x.shape
    K = w.shape[0]
    s = 2
    L = (T - K) // s + 1
    h = torch.einsum('blc,cd->bld', x[:, 0:s * L:s, :], w[0])
    for i in range(1, K):
        h = h + torch.einsum('blc,cd->bld', x[:, i:i + s * L:s, :], w[i])
    mu = torch.einsum('bld->bd', h)[:, None, :] / L
    var = torch.einsum('bld->bd', (h - mu) ** 2)[:, None, :] / L
    h = (h - mu) / torch.sqrt(var + 1e-5)
    h = h * gn_gamma[None, None, :] + gn_beta[None, None, :]
    return _gelu(h)
```

```cpp
#include <hip/hip_runtime.h>
#include <hip/hip_bf16.h>

constexpr int B_=2, T_=16000, D_=256, K_=31, L_=7985;

__device__ __forceinline__ float bf2f(unsigned short u){ return __uint_as_float(((unsigned)u)<<16); }

__global__ void conv_naive(const unsigned short* __restrict__ x, const unsigned short* __restrict__ w, float* __restrict__ h){
  int d = threadIdx.x;
  int row0 = blockIdx.x*8;
  int b = blockIdx.y;
  float acc[8]={0,0,0,0,0,0,0,0};
  for(int k=0;k<K_*D_;++k){
    float wv = bf2f(w[k*D_+d]);
    #pragma unroll
    for(int r=0;r<8;++r){
      int l=row0+r; if(l>=L_) l=L_-1;
      acc[r] += bf2f(x[((size_t)b*T_+2*l)*D_+k])*wv;
    }
  }
  for(int r=0;r<8;++r){int l=row0+r; if(l<L_) h[((size_t)b*L_+l)*D_+d]=acc[r];}
}
__global__ void stats_naive(const float* __restrict__ h, float* __restrict__ st){
  int d = blockIdx.x, b = blockIdx.y;
  __shared__ float sm[256];
  float s=0; for(int l=threadIdx.x;l<L_;l+=256) s+=h[((size_t)b*L_+l)*D_+d];
  sm[threadIdx.x]=s; __syncthreads();
  for(int o=128;o>0;o>>=1){ if(threadIdx.x<o) sm[threadIdx.x]+=sm[threadIdx.x+o]; __syncthreads(); }
  float mu=sm[0]/L_; __syncthreads();
  s=0; for(int l=threadIdx.x;l<L_;l+=256){ float v=h[((size_t)b*L_+l)*D_+d]-mu; s+=v*v; }
  sm[threadIdx.x]=s; __syncthreads();
  for(int o=128;o>0;o>>=1){ if(threadIdx.x<o) sm[threadIdx.x]+=sm[threadIdx.x+o]; __syncthreads(); }
  if(threadIdx.x==0){ st[(b*D_+d)*2]=mu; st[(b*D_+d)*2+1]=rsqrtf(sm[0]/L_+1e-5f); }
}
__global__ void norm_naive(const float* __restrict__ h, const float* __restrict__ st, const unsigned short* __restrict__ g, const unsigned short* __restrict__ be, __hip_bfloat16* __restrict__ out){
  size_t i=(size_t)blockIdx.x*256+threadIdx.x;
  if(i>=(size_t)B_*L_*D_) return;
  int d=i%D_; int b=i/((size_t)L_*D_);
  float y=(h[i]-st[(b*D_+d)*2])*st[(b*D_+d)*2+1]*bf2f(g[d])+bf2f(be[d]);
  float u=0.7978845608028654f*(y+0.044715f*y*y*y);
  out[i]=__float2bfloat16(0.5f*y*(1.f+tanhf(u)));
}

extern "C" void kernel_launch(void* const* d_in, const int* in_sizes, int n_in,
                              void* d_out, int out_size, void* d_ws, size_t ws_size,
                              hipStream_t stream) {
  const unsigned short* x=(const unsigned short*)d_in[0];
  const unsigned short* w=(const unsigned short*)d_in[1];
  const unsigned short* g=(const unsigned short*)d_in[2];
  const unsigned short* be=(const unsigned short*)d_in[3];
  float* h=(float*)d_ws; float* st=h+(size_t)B_*L_*D_;
  conv_naive<<<dim3((L_+7)/8,B_),256,0,stream>>>(x,w,h);
  stats_naive<<<dim3(D_,B_),256,0,stream>>>(h,st);
  norm_naive<<<(B_*L_*D_+255)/256,256,0,stream>>>(h,st,g,be,(__hip_bfloat16*)d_out);
}
```

```cpp
#include <hip/hip_runtime.h>
#include <hip/hip_bf16.h>

#define CODE_KB 22
#define ROTS 2u

typedef float f32x4 __attribute__((ext_vector_type(4)));
typedef float f32x2 __attribute__((ext_vector_type(2)));
typedef __bf16 bf16x2 __attribute__((ext_vector_type(2)));
typedef unsigned u32x4 __attribute__((ext_vector_type(4)));
typedef unsigned u32x2 __attribute__((ext_vector_type(2)));
typedef unsigned long long u64;
typedef __amdgpu_buffer_rsrc_t rsrc_t;
#define GAS __attribute__((address_space(1)))
#define RLX_AGENT __ATOMIC_RELAXED, __HIP_MEMORY_SCOPE_AGENT
#define DI __device__ __forceinline__

constexpr int T_ = 16000, L_ = 7985, TILE = 250;
constexpr unsigned WSEC = 16384u, XBASE = 98304u, XBUF = 17408u, GEN_OFF = XBASE + 3u * XBUF, LDS_BYTES = GEN_OFF + 256u, AB_OFF = XBASE;
constexpr unsigned X_BYTES = 2u * 16000u * 512u, W_BYTES = 31u * 256u * 512u;
constexpr unsigned WS_FLAGS = 4096u, WS_GRAN = 8192u, WS_WSUM = 1u << 20, WSUM_BYTES = 4u << 20, WS_XSUM = 8u << 20, XS_TILE = 8u * XBUF;

DI unsigned rfl(unsigned v) { return __builtin_amdgcn_readfirstlane(v); }
DI unsigned cvtpk(float lo, float hi) { f32x2 v = {lo, hi}; bf16x2 b = __builtin_convertvector(v, bf16x2); return __builtin_bit_cast(unsigned, b); }
DI float bflo(unsigned u) { return __uint_as_float(u << 16); }
DI float bfhi(unsigned u) { return __uint_as_float(u & 0xffff0000u); }
#define DMA4(P, ldsaddr, v0, v1, v2, v3, srd, soff) \
  asm volatile("s_mov_b32 m0, %0\n\ts_nop 0\n\tbuffer_load_dwordx4 %1, %5, %6 offen" P " lds\n\tbuffer_load_dwordx4 %2, %5, %6 offen offset:1024" P " lds\n\t" \
               "buffer_load_dwordx4 %3, %5, %6 offen offset:2048" P " lds\n\tbuffer_load_dwordx4 %4, %5, %6 offen offset:3072" P " lds" \
               :: "s"(ldsaddr), "v"(v0), "v"(v1), "v"(v2), "v"(v3), "s"(srd), "s"(soff) : "memory")
#define DMA2(P, ldsaddr, v0, v1, srd, soff) \
  asm volatile("s_mov_b32 m0, %0\n\ts_nop 0\n\tbuffer_load_dwordx4 %1, %3, %4 offen" P " lds\n\tbuffer_load_dwordx4 %2, %3, %4 offen offset:1024" P " lds" \
               :: "s"(ldsaddr), "v"(v0), "v"(v1), "s"(srd), "s"(soff) : "memory")
#define DMAX(P, ldsaddr, v0, v1, v2, srd, soff) \
  asm volatile("s_mov_b32 m0, %0\n\ts_nop 0\n\tbuffer_load_dwordx4 %1, %4, %5 offen" P " lds\n\tbuffer_load_dwordx4 %2, %4, %5 offen offset:1024" P " lds\n\t" \
               "s_mov_b64 exec, 0xff\n\tbuffer_load_dwordx4 %3, %4, %5 offen offset:2048" P " lds\n\ts_mov_b64 exec, -1" \
               :: "s"(ldsaddr), "v"(v0), "v"(v1), "v"(v2), "s"(srd), "s"(soff) : "memory")
#define IN ""
#define DEV " sc1"
#define WAITV(n) asm volatile("s_waitcnt vmcnt(%0)" :: "i"(n) : "memory")
#define BAR() asm volatile("s_barrier" ::: "memory")
#define LBAR() asm volatile("s_waitcnt lgkmcnt(0)\n\ts_barrier" ::: "memory")
#define WAITBAR(n) asm volatile("s_waitcnt vmcnt(%0)\n\ts_barrier" :: "i"(n) : "memory")
#define PIN(x) asm volatile("" : "+s"(x))
DI unsigned ldsw(unsigned a) { unsigned d; asm volatile("ds_read_b32 %0, %1\n\ts_waitcnt lgkmcnt(0)" : "=&v"(d) : "v"(a) : "memory"); return rfl(d); }
DI float gelu_tanh(float y) {
  const float c1 = -2.0f * 0.7978845608028654f * 1.4426950408889634f;
  const float c2 = c1 * 0.044715f;
  const float z = y * __builtin_fmaf(y * y, c2, c1);
  return y * __builtin_amdgcn_rcpf(1.0f + __builtin_amdgcn_exp2f(z));
}
DI float fadd_s(float a, float b) { float r; asm("v_add_f32_e32 %0, %1, %2" : "=v"(r) : "v"(a), "v"(b)); return r; }
DI u32x4 add_bf16x8(u32x4 a, u32x4 b) {
  u32x4 o;
#pragma unroll
  for (int i = 0; i < 4; ++i) o[i] = cvtpk(fadd_s(bflo(a[i]), bflo(b[i])), fadd_s(bfhi(a[i]), bfhi(b[i])));
  return o;
}

__global__ __launch_bounds__(512, 2) void conv_gn_gelu(const unsigned short* x, const unsigned short* w,
                                                    const unsigned short* gamma, const unsigned short* beta,
                                                    unsigned short* out, char* ws) {
  __shared__ __attribute__((aligned(1024))) char smem[LDS_BYTES];
  unsigned long long ka0, ka1, ka2, ka3, ka4, ka5;
  {
    const void* kp = (const void*)__builtin_amdgcn_kernarg_segment_ptr();
    asm volatile("s_load_dwordx2 %0, %6, 0x0\n\ts_load_dwordx2 %1, %6, 0x8\n\ts_load_dwordx2 %2, %6, 0x10\n\ts_load_dwordx2 %3, %6, 0x18\n\ts_load_dwordx2 %4, %6, 0x20\n\ts_load_dwordx2 %5, %6, 0x28"
                 : "=&s"(ka0), "=&s"(ka1), "=&s"(ka2), "=&s"(ka3), "=&s"(ka4), "=&s"(ka5) : "s"(kp));
  }
  const unsigned tid = threadIdx.x, lane = tid & 63u, wv = rfl(tid >> 6), lw = wv & 3u;
  u64 pc = 0;
  {
    asm volatile("s_getpc_b64 %0" : "=s"(pc));
    if (tid < CODE_KB * 8u) asm volatile("global_load_dword a119, %0, %1" :: "v"(tid * 128u), "s"(pc & ~127ull) : "memory", "a119");
  }
  const unsigned bid = blockIdx.x, xcd = bid & 7u, idx = bid >> 3, mtg = xcd * 8u + (idx >> 2), nt = idx & 3u;
  const unsigned b = mtg >> 5, mt = mtg & 31u, l0 = mt * TILE, n0 = nt * 64u, row0 = b * T_ + 2u * l0;
  const unsigned ldsb = (unsigned)(size_t)smem, rot = (nt + ROTS) & 3u;
  const unsigned q = lane >> 4, r = lane & 15u, rr = lane >> 3;
  auto woff = [&](unsigned e) { return (8u * e + rr) * 512u + (((lane & 7u) ^ ((rr & 2u) | ((e & 1u) << 2))) << 4) + n0 * 2u; };
#define WOFF(kk) (((0x101302u >> (((kk) >> 4) * 4u)) & 3u) * 131072u + ((kk) & 1u) * 2097152u + ((((kk) >> 1) + 2u * rot) & 7u) * 16384u)
  unsigned vw[4], xa = rr * 16384u + (((lane & 7u) ^ (rr >> 1)) << 4), xb = (xa ^ 64u) + 130048u, xc = 260096u + ((lane & 7u) << 4);
#pragma unroll
  for (unsigned e = 0; e < 4u; ++e) vw[e] = woff(e) - e * 1024u;
  asm volatile("" : "+v"(vw[0]), "+v"(vw[1]), "+v"(vw[2]), "+v"(vw[3]), "+v"(xa), "+v"(xb), "+v"(xc));
  asm volatile("s_waitcnt lgkmcnt(0)" : "+s"(ka0), "+s"(ka1), "+s"(ka2), "+s"(ka3), "+s"(ka4), "+s"(ka5));
  x = (const unsigned short*)ka0; w = (const unsigned short*)ka1; out = (unsigned short*)ka4; ws = (char*)ka5;
  const rsrc_t srdX = __builtin_amdgcn_make_buffer_rsrc((void*)(x + (size_t)row0 * 256u), (short)0, (int)((T_ - 1u - 2u * l0) * 512u), 0x00020000);
  const rsrc_t srdW = __builtin_amdgcn_make_buffer_rsrc((void*)w, (short)0, (int)W_BYTES, 0x00020000);
  const rsrc_t srdC = __builtin_amdgcn_make_buffer_rsrc((void*)ws, (short)0, (int)WS_GRAN, 0x00020000);
  {
    DMA2(IN, ldsb + wv * 2048u, vw[0], vw[1], srdW, WOFF(0u) + (wv >> 1) * 524288u + (wv & 1u) * 8192u);
    DMAX(IN, ldsb + XBASE + wv * 2176u, xa, xb, xc, srdX, wv * 2048u + rot * 128u);
    DMAX(IN, ldsb + XBASE + XBUF + wv * 2176u, xa, xb, xc, srdX, wv * 2048u + ((rot + 1u) & 3u) * 128u);
  }
  const unsigned c8 = lane & 7u;
  const int nvalid = min(TILE, L_ - (int)l0);
  const unsigned grp = (b * 4u + nt) * 4u + lw;

  unsigned short* op;
  unsigned abo, hva;
  f32x4 hv[4][2];
#define EPI_CONST() do { op = out + ((size_t)b * L_ + l0 + 32u * wv + rr) * 256u + n0 + c8 * 8u; abo = AB_OFF + c8 * 32u; \
    hva = (32u * wv + rr) * 256u + (((2u * c8) ^ (2u * wv) ^ (rr & 1u)) << 4); asm volatile("" : "+v"(op), "+v"(abo), "+v"(hva)); } while (0)
#define HV_READ() _Pragma("unroll") for (int i = 0; i < 4; ++i) { \
    hv[i][0] = *(const f32x4*)(smem + ((hva ^ ((i >> 1) << 4)) + i * 2048u)); hv[i][1] = *(const f32x4*)(smem + ((hva ^ (((i >> 1) ^ 1) << 4)) + i * 2048u)); }

  if (wv >= 4u) {
    const rsrc_t srdWS = __builtin_amdgcn_make_buffer_rsrc((void*)(ws + WS_WSUM), (short)0, (int)WSUM_BYTES, 0x00020000);
    const rsrc_t srdXS = __builtin_amdgcn_make_buffer_rsrc((void*)(ws + WS_XSUM + (size_t)mtg * XS_TILE), (short)0, (int)XS_TILE, 0x00020000);
    DMA4(IN, ldsb + WSEC + lw * 4096u, vw[0], vw[1], vw[2], vw[3], srdW, WOFF(1u) + lw * 524288u);
    DMA4(IN, ldsb + 2u * WSEC + lw * 4096u, vw[0], vw[1], vw[2], vw[3], srdW, WOFF(2u) + lw * 524288u);
    if (lw >= 2u) { asm volatile("buffer_load_dword a119, %0, %1, %2 offen" :: "v"(lane * 128u), "s"(srdW), "s"((bid * 2u + (lw & 1u)) * 8192u) : "memory", "a119"); WAITV(12); } else
    WAITV(11);
    BAR();
    DMA4(IN, ldsb + 3u * WSEC + lw * 4096u, vw[0], vw[1], vw[2], vw[3], srdW, WOFF(3u) + lw * 524288u);
    if (lw < 3u) {
      const unsigned wlim = ldsb + 5u * WSEC, lin = lane * 16u, linw = lin + nt * 4096u;
      unsigned wm = ldsb + 4u * WSEC + lw * 4096u, wc = 2u * 131072u + lw * 524288u, cb = ((2u + 2u * rot) & 7u) * 16384u, wo = wc + cb;
#define WADV(p) do { wm = (wm >= wlim) ? wm - 5u * WSEC : wm + WSEC; if ((p) & 1) { cb = (cb + 16384u) & 0x1ffffu; wo = wc + cb; } else wo += 2097152u; PIN(wm); PIN(wo); } while (0)
#define WCLASS() do { if ((ch & 3u) == 0u) { wc = ((0x101302u >> ch) & 3u) * 131072u + lw * 524288u; } } while (0)
#define WRAW(m, o) DMA4(IN, m, vw[0], vw[1], vw[2], vw[3], srdW, o)
#define WSUM(m, o) DMA4(DEV, m, linw, linw, linw, linw, srdWS, o)
      unsigned ch = 2u;
      if (lw == 0u) {
#define L0BODY(WLD, full) do { \
          WLD(wm, wo); WLD(wm + 12288u, wo + 1572864u); WADV(0); WAITBAR(16); \
          WLD(wm, wo); if (full) WLD(wm + 12288u, wo + 1572864u); WADV(1); WAITBAR(16); \
          WLD(wm, wo); WLD(wm + 12288u, wo + 1572864u); WADV(2); WAITBAR(16); \
          WLD(wm, wo); if (full) WLD(wm + 12288u, wo + 1572864u); WCLASS(); WADV(3); WAITBAR(16); } while (0)
#pragma unroll 1
        for (; ch < 17u; ++ch) L0BODY(WRAW, (ch - 9u) >= 4u);
#pragma unroll 1
        for (; ch < 25u; ++ch) L0BODY(WSUM, true);
        WAITBAR(16); WAITBAR(8); WAITBAR(0);
      } else {
        const unsigned ub = (lw >> 1) * 4u;
        unsigned xm = ldsb + XBASE + 2u * XBUF + ub * 2176u, xo = ((rot + 2u) & 3u) * 128u + ub * 2048u;
#define XRAW(p) DMAX(IN, xm + 2176u * (p), xa, xb, xc, srdX, xo + 2048u * (p))
#define XSM(p) DMAX(DEV, xm + 2176u * (p), lin, lin, lin, srdXS, xo + 2176u * (p))
#define L1BODY(XLD, WLD, first) do { \
          XLD(0); WLD(wm, wo); WADV(0); if (first) WAITV(15); WAITBAR(18); \
          XLD(1); WLD(wm, wo); WADV(1); WAITBAR(18); \
          XLD(2); WLD(wm, wo); WADV(2); WAITBAR(18); \
          XLD(3); WLD(wm, wo); WCLASS(); WADV(3); \
          xm = (xm >= ldsb + XBASE + 2u * XBUF) ? xm - 2u * XBUF : xm + XBUF; \
          const unsigned cn = ch + 1u; \
          xo = (((0x103120u >> (cn & 28u)) & 3u) * 4u + ((cn + rot) & 3u)) * (cn < 16u ? 128u : XBUF) + ub * (cn < 16u ? 2048u : 2176u); \
          PIN(xm); PIN(xo); WAITBAR(18); } while (0)
#pragma unroll 1
        for (; ch < 16u; ++ch) L1BODY(XRAW, WRAW, ch == 2u);
        L1BODY(XSM, WRAW, false); ++ch;
#pragma unroll 1
        for (; ch < 24u; ++ch) L1BODY(XSM, WSUM, false);
        WSUM(wm, wo); WADV(0); WAITBAR(12);
        WSUM(wm, wo); WADV(1); WAITBAR(12);
        WSUM(wm, wo); WADV(2); WAITBAR(12);
        WSUM(wm, wo); WAITBAR(12);
        WAITBAR(8); WAITBAR(4); WAITBAR(0);
      }
    } else {
      const unsigned wi = mtg * 4u + nt, ap = wi >> 4, tb = (ap >> 1) * 524288u + (ap & 1u) * 131072u, wli = tb + (wi & 15u) * 8192u, wso = tb + ((wi & 15u) >> 1) * 16384u + (wi & 1u) * 2048u;
      const unsigned wvo = (rr & 3u) * 4096u + (lane >> 5) * 128u + (lane & 7u) * 16u;
      const unsigned fo1 = WS_FLAGS + 1024u + (((nt + lane) & 3u) * 64u + mtg) * 4u, l16 = lane * 16u;
      u32x4 ha[8], hb[8], A[17], Bq[17];
      asm volatile("s_mov_b32 m0, %0\n\ts_nop 0\n\tbuffer_load_dword off, %1, 0 sc1 lds" :: "s"(ldsb + GEN_OFF), "s"(srdC) : "memory");
      u32x4 f0; unsigned f1;
#define HLOAD(i) asm volatile("buffer_load_dwordx4 %0, %2, %3, %4 offen\n\tbuffer_load_dwordx4 %1, %2, %3, %5 offen" : "=&v"(ha[i]), "=&v"(hb[i]) : "v"(l16), "s"(srdW), "s"(wli + (i) * 1024u), "s"(wli + (i) * 1024u + 262144u) : "memory")
#define HSUM(i) do { asm volatile("" : "+v"(ha[i]), "+v"(hb[i])); const u32x4 o = add_bf16x8(ha[i], hb[i]); \
        asm volatile("buffer_store_dwordx4 %0, %1, %2, %3 offen sc1\n\ts_nop 1" :: "v"(o), "v"(wvo ^ (((((i) & 1) << 1) | ((i) & 4)) << 4)), "s"(srdWS), "s"(wso + (i) * 256u) : "memory"); } while (0)
#define XSUM(i) do { const u32x4 o = add_bf16x8(A[i], Bq[i]); \
        asm volatile("buffer_store_dwordx4 %0, %1, %2, %3 offen sc1\n\ts_nop 1" :: "v"(o), "v"(l16), "s"(srdXS), "s"(so + (i) * 1024u) : "memory"); } while (0)
#define RDA(i) A[i] = *(const u32x4*)(pa + (i) * 1024)
#define RDB(i) Bq[i] = *(const u32x4*)(pbq + (i) * 1024)
#define FLAG(s) do { const unsigned tg_ = ldsw(ldsb + GEN_OFF) + 1u; \
        asm volatile("s_mov_b64 exec, 1\n\tbuffer_store_dword %0, off, %1, %2 sc1\n\ts_mov_b64 exec, -1" :: "v"(tg_), "s"(srdC), "s"(WS_FLAGS + (s) * 1024u + (nt * 64u + mtg) * 4u) : "memory"); } while (0)
#define POLL0_ISSUE() asm volatile("buffer_load_dwordx4 %0, %1, %2, %3 offen sc1" : "=&v"(f0) : "v"(l16), "s"(srdC), "s"(WS_FLAGS) : "memory")
#define POLL0_CHECK(on) do { \
        asm volatile("" : "+v"(f0)); const unsigned tg_ = ldsw(ldsb + GEN_OFF) + 1u; \
        _Pragma("unroll 1") for (unsigned spin = 0; (on) && !__all(f0[0] == tg_ && f0[1] == tg_ && f0[2] == tg_ && f0[3] == tg_) && spin < (1u << 16); ++spin) {     \
          POLL0_ISSUE(); asm volatile("s_waitcnt vmcnt(0)" : "+v"(f0) :: "memory"); \
        } \
        if ((on) && bid == 0u) asm volatile("s_mov_b64 exec, 1\n\tbuffer_store_dword %0, off, %1, 0 sc1\n\ts_mov_b64 exec, -1" :: "v"(tg_), "s"(srdC) : "memory");     \
        } while (0)
#define POLL1_ISSUE() asm volatile("buffer_load_dword %0, %1, %2, 0 offen sc1" : "=&v"(f1) : "v"(fo1), "s"(srdC) : "memory")
#define POLL1_CHECK(j) do {     \
        asm volatile("" : "+v"(f1)); const unsigned tg_ = ldsw(ldsb + GEN_OFF) + 1u; \
        _Pragma("unroll 1") for (unsigned spin = 0; !__all(f1 == tg_ || (lane & 3u) > (j)) && spin < (1u << 16); ++spin) { POLL1_ISSUE(); asm volatile("s_waitcnt vmcnt(0)" : "+v"(f1) :: "memory"); } \
        } while (0)
      const unsigned tvx = (lane >> 2) * 2048u + (lane & 3u) * 128u, tvx8 = tvx + (lane >> 3) * 0x1000000u, tvw = (lane & 31u) * 128u + (lane >> 5) * 0x40000000u;
#define XT(cls, p, off) asm volatile("buffer_load_dword a119, %0, %1, %2 offen" "" :: "v"((p) == 2u ? tvx8 : tvx), "s"(srdX), "s"(nt * 69632u + (cls) * 512u + (p) * 32768u + (off)) : "memory", "a119")
#define WT(cls, off) asm volatile("buffer_load_dword a119, %0, %1, %2 offen" :: "v"(tvw), "s"(srdW), "s"((bid >> 5) * 524288u + (cls) * 131072u + (bid & 31u) * 4096u + (off)) : "memory", "a119")
#define CODE_AGAIN() do { _Pragma("unroll") for (unsigned e = 0; e < (CODE_KB * 8u + 63u) / 64u; ++e) asm volatile("global_load_dword a119, %0, %1" :: "v"(min(lane + 64u * e, CODE_KB * 8u - 1u) * 128u), "s"(pc & ~127ull) : "memory", "a119"); } while (0)
#define BARS(n) _Pragma("unroll 1") for (int i_ = 0; i_ < (n); ++i_) BAR()
      const char* pa = smem + XBASE + l16;
      WAITV(9); BAR();
      RDA(0); RDA(1); RDA(2); RDA(3); RDA(4); RDA(5); RDA(6); RDA(7); RDA(8); RDA(9); RDA(10); RDA(11); RDA(12); RDA(13); RDA(14); RDA(15); RDA(16); WAITV(5); XT(2u, 0u, 0u); XT(2u, 1u, 0u); XT(2u, 2u, 0u); LBAR();
      WAITV(3); BAR();
       BAR();
       BAR();
       XT(1u, 0u, 0u); XT(1u, 1u, 0u); XT(1u, 2u, 0u); BAR();
       BAR();
       BAR();
       BAR();
       XT(3u, 0u, 0u); XT(3u, 1u, 0u); XT(3u, 2u, 0u); BAR();
       BAR();
#pragma unroll 1
      for (unsigned s = 0; s < 2u; ++s) {
      const char* pbq = smem + XBASE + (s ^ 1u) * XBUF + l16; const unsigned so = (4u * s + rot) * XBUF;
      RDB(0); XSUM(0); BAR();
      RDB(1); XSUM(1); BAR();
      RDB(9); XSUM(9); BAR();
      RDB(2); XSUM(2); BAR();
      RDB(3); XSUM(3); BAR();
      RDB(4); XSUM(4); BAR();
      RDB(5); XSUM(5); BAR();
      RDB(6); RDB(7); RDB(8); RDB(10); RDB(11); RDB(12); RDB(13); RDB(14); RDB(15); RDB(16); XSUM(6); LBAR();
      XSUM(7); BAR();
      XSUM(8); BAR();
      XSUM(10); BAR();
      XSUM(11); BAR();
      POLL0_ISSUE(); XSUM(12); BAR();
      XSUM(13); BAR();
      XSUM(14); BAR();
      XSUM(15); BAR();
      WAITV(4); POLL0_CHECK(s); XSUM(16); BAR();
      if (s == 0u) {
      pa = smem + XBASE + 2u * XBUF + l16;
      HLOAD(0); HLOAD(1);  BAR();
      HLOAD(2); HLOAD(3);  BAR();
      RDA(0); RDA(1); RDA(2); RDA(3); RDA(4); RDA(5); RDA(6); RDA(7); RDA(8); RDA(9); RDA(10); RDA(11); RDA(12); RDA(13); RDA(14); RDA(15); RDA(16); LBAR();
      WAITV(6); HSUM(0); HLOAD(4);  BAR();
      WAITV(7); HSUM(1); HLOAD(5);  BAR();
      WAITV(8); HSUM(2); HLOAD(6);  BAR();
      WAITV(9); HSUM(3); HLOAD(7);  BAR();
      WAITV(9); HSUM(4);  BAR();
      WAITV(7); HSUM(5);  BAR();
      WAITV(5); HSUM(6);  BAR();
      WAITV(3); HSUM(7);  BAR();
       BAR();
       BAR();
       BAR();
      WAITV(0); FLAG(0u);  BAR();
      } }
      BARS(3);
      WAITV(0); FLAG(1u); BAR();
      BARS(8);
#pragma unroll 1
      for (unsigned j = 1; j < 4u; ++j) { POLL1_ISSUE(); BARS(3); WAITV(0); POLL1_CHECK(j); BAR(); }
      BARS(8); CODE_AGAIN(); BARS(3);
    }
    EPI_CONST();
    const unsigned tag = ldsw(ldsb + GEN_OFF) + 1u;
    const unsigned gmu = ((const GAS unsigned short*)ka2)[n0 + 16u * lw + r], btu = ((const GAS unsigned short*)ka3)[n0 + 16u * lw + r];
    unsigned abw = ldsb + AB_OFF + (q & 1u) * 256u + (16u * lw + r) * 4u, go = lane * 16u, go2 = go + 4096u;
    asm volatile("" : "+v"(abw), "+v"(go), "+v"(go2));
    const rsrc_t srdG = __builtin_amdgcn_make_buffer_rsrc((void*)(ws + WS_GRAN + (size_t)grp * 8192u), (short)0, 8192, 0x00020000);
    u32x4 g[8];
    BAR();
    __builtin_amdgcn_s_sleep(9);
#pragma unroll
    for (int i = 0; i < 8; ++i) g[i] = __builtin_amdgcn_raw_buffer_load_b128(srdG, (i < 4 ? go : go2) + (i & 3) * 1024u, 0, 16);
    BAR();
    HV_READ();
#pragma unroll 1
    for (unsigned spin = 0;; ++spin) {
      unsigned bad[8], any = 0u;
#pragma unroll
      for (int i = 0; i < 8; ++i) { bad[i] = (g[i][1] ^ tag) | (g[i][3] ^ tag); asm volatile("" : "+v"(bad[i])); any |= bad[i]; }
      if (__builtin_amdgcn_ballot_w64(any != 0u) == 0ull || spin > (1u << 16)) break;
      __builtin_amdgcn_s_sleep(1);
      asm volatile("" ::: "memory");
#pragma unroll
      for (int i = 0; i < 8; ++i) if (__builtin_amdgcn_ballot_w64(bad[i] != 0u) != 0ull) g[i] = __builtin_amdgcn_raw_buffer_load_b128(srdG, (i < 4 ? go : go2) + (i & 3) * 1024u, 0, 16);
    }
    float S1 = (__uint_as_float(g[0][0]) + __uint_as_float(g[1][0])) + (__uint_as_float(g[2][0]) + __uint_as_float(g[3][0])) + ((__uint_as_float(g[4][0]) + __uint_as_float(g[5][0])) + (__uint_as_float(g[6][0]) + __uint_as_float(g[7][0])));
    float S2 = (__uint_as_float(g[0][2]) + __uint_as_float(g[1][2])) + (__uint_as_float(g[2][2]) + __uint_as_float(g[3][2])) + ((__uint_as_float(g[4][2]) + __uint_as_float(g[5][2])) + (__uint_as_float(g[6][2]) + __uint_as_float(g[7][2])));
    {
      auto a = __builtin_amdgcn_permlane32_swap(__float_as_uint(S1), __float_as_uint(S2), false, false);
      const float m = __uint_as_float(a[0]) + __uint_as_float(a[1]);
      auto d = __builtin_amdgcn_permlane16_swap(__float_as_uint(m), __float_as_uint(m), false, false);
      const float m2 = __uint_as_float(d[0]) + __uint_as_float(d[1]);
      auto e = __builtin_amdgcn_permlane32_swap(__float_as_uint(m2), __float_as_uint(m2), false, false);
      S1 = __uint_as_float(e[0]); S2 = __uint_as_float(e[1]);
    }
    const float mean = S1 * (1.0f / L_);
    const float var = fmaxf(S2 * (1.0f / L_) - mean * mean, 0.f);
    const float sc = __builtin_amdgcn_rsqf(var + 1e-5f) * bflo(gmu), sh = bflo(btu) - mean * sc;
    asm volatile("ds_write_b32 %0, %1" :: "v"(abw), "v"((q & 1u) ? sh : sc) : "memory");
  } else {
    const unsigned ri = (r + 4u) & 15u, pr = ((ri << 1) | (ri >> 3)) & 15u;
    {
      const unsigned qq = r >> 2, p4 = lane & 3u, k = 8u * q + qq, c = 2u * wv + (p4 >> 1);
      const unsigned vW0 = ldsb + k * 128u + ((c ^ ((k & 2u) | ((k >> 1) & 4u))) << 4) + 8u * (p4 & 1u);
      const unsigned v00 = ldsb + XBASE + pr * 128u + ((q ^ ((pr >> 1) & 7u)) << 4);
      const unsigned v10 = ldsb + XBASE + (pr + 1u) * 128u + ((q ^ (((pr + 1u) >> 1) & 7u)) << 4);
      const int slim = nvalid - 16 * (int)pr;
      const u64 mA = __ballot(slim > 9), mB = __ballot(slim > 10), mC = __ballot(slim > 15);
      const rsrc_t srdG = __builtin_amdgcn_make_buffer_rsrc((void*)(ws + WS_GRAN + (size_t)grp * 8192u + mt * 256u), (short)0, 256, 0x00020000);
      unsigned t0, t1, t2, s0, s1, s2, s3;
      EPI_CONST();
      __builtin_amdgcn_s_setprio(2);
      asm volatile(
      "v_mov_b32 v48, 0\n\t"
      "v_mov_b32 v49, 0\n\t"
      "v_mov_b32 v50, 0\n\t"
      "v_mov_b32 v51, 0\n\t"
      "v_mov_b32 v52, 0\n\t"
      "v_mov_b32 v53, 0\n\t"
      "v_mov_b32 v54, 0\n\t"
      "v_mov_b32 v55, 0\n\t"
      "v_mov_b32 v56, 0\n\t"
      "v_mov_b32 v57, 0\n\t"
      "v_mov_b32 v58, 0\n\t"
      "v_mov_b32 v59, 0\n\t"
      "v_mov_b32 v60, 0\n\t"
      "v_mov_b32 v61, 0\n\t"
      "v_mov_b32 v62, 0\n\t"
      "v_mov_b32 v63, 0\n\t"
      "v_mov_b32 v64, 0\n\t"
      "v_mov_b32 v65, 0\n\t"
      "v_mov_b32 v66, 0\n\t"
      "v_mov_b32 v67, 0\n\t"
      "v_mov_b32 v68, 0\n\t"
      "v_mov_b32 v69, 0\n\t"
      "v_mov_b32 v70, 0\n\t"
      "v_mov_b32 v71, 0\n\t"
      "v_mov_b32 v72, 0\n\t"
      "v_mov_b32 v73, 0\n\t"
      "v_mov_b32 v74, 0\n\t"
      "v_mov_b32 v75, 0\n\t"
      "v_mov_b32 v76, 0\n\t"
      "v_mov_b32 v77, 0\n\t"
      "v_mov_b32 v78, 0\n\t"
      "v_mov_b32 v79, 0\n\t"
      "v_mov_b32 v80, 0\n\t"
      "v_mov_b32 v81, 0\n\t"
      "v_mov_b32 v82, 0\n\t"
      "v_mov_b32 v83, 0\n\t"
      "v_mov_b32 v84, 0\n\t"
      "v_mov_b32 v85, 0\n\t"
      "v_mov_b32 v86, 0\n\t"
      "v_mov_b32 v87, 0\n\t"
      "v_mov_b32 v88, 0\n\t"
      "v_mov_b32 v89, 0\n\t"
      "v_mov_b32 v90, 0\n\t"
      "v_mov_b32 v91, 0\n\t"
      "v_mov_b32 v92, 0\n\t"
      "v_mov_b32 v93, 0\n\t"
      "v_mov_b32 v94, 0\n\t"
      "v_mov_b32 v95, 0\n\t"
      "v_mov_b32 v96, 0\n\t"
      "v_mov_b32 v97, 0\n\t"
      "v_mov_b32 v98, 0\n\t"
      "v_mov_b32 v99, 0\n\t"
      "v_mov_b32 v100, 0\n\t"
      "v_mov_b32 v101, 0\n\t"
      "v_mov_b32 v102, 0\n\t"
      "v_mov_b32 v103, 0\n\t"
      "v_mov_b32 v104, 0\n\t"
      "v_mov_b32 v105, 0\n\t"
      "v_mov_b32 v106, 0\n\t"
      "v_mov_b32 v107, 0\n\t"
      "v_mov_b32 v108, 0\n\t"
      "v_mov_b32 v109, 0\n\t"
      "v_mov_b32 v110, 0\n\t"
      "v_mov_b32 v111, 0\n\t"
      "v_mov_b32 v16, 0\n\t"
      "v_mov_b32 v17, 0\n\t"
      "v_mov_b32 v18, 0\n\t"
      "v_mov_b32 v19, 0\n\t"
      "v_mov_b32 v20, 0\n\t"
      "v_mov_b32 v21, 0\n\t"
      "v_mov_b32 v22, 0\n\t"
      "v_mov_b32 v23, 0\n\t"
      "v_mov_b32 %0, %7\n\t"
      "v_xor_b32 %1, 64, %8\n\t"
      "v_mov_b32 %2, %9\n\t"
      "s_mov_b32 %3, 0\n\t"
      "s_mov_b32 %4, 17408\n\t"
      "s_mov_b32 %5, 4\n\t"
      "s_mov_b32 %6, 0\n\t"
      "s_waitcnt vmcnt(3) lgkmcnt(0)\n\t"
      "s_barrier\n\t"
      "ds_read_b64_tr_b16 v[28:29], %7 offset:0\n\t"
      "ds_read_b64_tr_b16 v[30:31], %7 offset:512\n\t"
      "ds_read_b64_tr_b16 v[32:33], %7 offset:4096\n\t"
      "ds_read_b64_tr_b16 v[34:35], %7 offset:4608\n\t"
      "ds_read_b128 a[0:3], %8 offset:0\n\t"
      "ds_read_b128 a[4:7], %8 offset:2176\n\t"
      "ds_read_b128 a[8:11], %8 offset:4352\n\t"
      "ds_read_b128 a[12:15], %8 offset:6528\n\t"
      "ds_read_b128 a[16:19], %8 offset:8704\n\t"
      "ds_read_b128 a[20:23], %8 offset:10880\n\t"
      "ds_read_b128 a[24:27], %8 offset:13056\n\t"
      "ds_read_b128 a[28:31], %8 offset:15232\n\t"
      "ds_read_b128 a[32:35], %9 offset:0\n\t"
      "ds_read_b128 a[36:39], %9 offset:2176\n\t"
      "ds_read_b128 a[40:43], %9 offset:4352\n\t"
      "s_waitcnt lgkmcnt(12)\n\t"
      "ds_read_b128 a[44:47], %9 offset:6528\n\t"
      "ds_read_b128 a[48:51], %9 offset:8704\n\t"
      "ds_read_b128 a[52:55], %9 offset:10880\n\t"
      "s_waitcnt lgkmcnt(12)\n\t"
      "ds_read_b128 a[56:59], %9 offset:13056\n\t"
      ".p2align 5\n\t"
      "LQ_%=:\n\t"
      "s_waitcnt lgkmcnt(3)\n\t"
      "v_mfma_f32_16x16x32_bf16 v[80:83], v[28:31], a[0:3], v[80:83]\n\t"
      "ds_read_b64_tr_b16 v[36:37], %0 offset:8192\n\t"
      "v_mfma_f32_16x16x32_bf16 v[80:83], v[32:35], a[4:7], v[80:83]\n\t"
      "ds_read_b64_tr_b16 v[38:39], %0 offset:8704\n\t"
      "v_mfma_f32_16x16x32_bf16 v[84:87], v[32:35], a[8:11], v[84:87]\n\t"
      "ds_read_b64_tr_b16 v[40:41], %0 offset:12288\n\t"
      "v_mfma_f32_16x16x32_bf16 v[84:87], v[28:31], a[4:7], v[84:87]\n\t"
      "ds_read_b64_tr_b16 v[42:43], %0 offset:12800\n\t"
      "v_mfma_f32_16x16x32_bf16 v[88:91], v[28:31], a[8:11], v[88:91]\n\t"
      "s_add_u32 %3, %3, 16384\n\t"
      "v_mfma_f32_16x16x32_bf16 v[88:91], v[32:35], a[12:15], v[88:91]\n\t"
      "ds_read_b128 a[60:63], %1 offset:0\n\t"
      "v_mfma_f32_16x16x32_bf16 v[92:95], v[32:35], a[16:19], v[92:95]\n\t"
      "s_cmp_eq_u32 %3, 98304\n\t"
      "v_mfma_f32_16x16x32_bf16 v[92:95], v[28:31], a[12:15], v[92:95]\n\t"
      "ds_read_b128 a[64:67], %1 offset:2176\n\t"
      "v_mfma_f32_16x16x32_bf16 v[96:99], v[28:31], a[16:19], v[96:99]\n\t"
      "s_cselect_b32 %3, 0, %3\n\t"
      "v_mfma_f32_16x16x32_bf16 v[96:99], v[32:35], a[20:23], v[96:99]\n\t"
      "ds_read_b128 a[68:71], %1 offset:4352\n\t"
      "v_mfma_f32_16x16x32_bf16 v[100:103], v[32:35], a[24:27], v[100:103]\n\t"
      "v_add_u32 %0, %3, %7\n\t"
      "v_mfma_f32_16x16x32_bf16 v[100:103], v[28:31], a[20:23], v[100:103]\n\t"
      "ds_read_b128 a[72:75], %1 offset:6528\n\t"
      "v_mfma_f32_16x16x32_bf16 v[104:107], v[28:31], a[24:27], v[104:107]\n\t"
      "v_xor_b32 %2, 64, %2\n\t"
      "v_mfma_f32_16x16x32_bf16 v[104:107], v[32:35], a[28:31], v[104:107]\n\t"
      "v_mfma_f32_16x16x32_bf16 v[108:111], v[32:35], a[32:35], v[108:111]\n\t"
      "v_mfma_f32_16x16x32_bf16 v[108:111], v[28:31], a[28:31], v[108:111]\n\t"
      "s_waitcnt vmcnt(0) lgkmcnt(4)\n\t"
      "s_barrier\n\t"
      "v_mfma_f32_16x16x32_bf16 v[108:111], v[36:39], a[36:39], v[108:111]\n\t"
      "ds_read_b64_tr_b16 v[28:29], %0 offset:0\n\t"
      "v_mfma_f32_16x16x32_bf16 v[108:111], v[40:43], a[40:43], v[108:111]\n\t"
      "ds_read_b64_tr_b16 v[30:31], %0 offset:512\n\t"
      "v_mfma_f32_16x16x32_bf16 v[104:107], v[40:43], a[36:39], v[104:107]\n\t"
      "ds_read_b64_tr_b16 v[32:33], %0 offset:4096\n\t"
      "v_mfma_f32_16x16x32_bf16 v[104:107], v[36:39], a[32:35], v[104:107]\n\t"
      "ds_read_b64_tr_b16 v[34:35], %0 offset:4608\n\t"
      "v_mfma_f32_16x16x32_bf16 v[100:103], v[36:39], a[28:31], v[100:103]\n\t"
      "v_mfma_f32_16x16x32_bf16 v[100:103], v[40:43], a[32:35], v[100:103]\n\t"
      "ds_read_b128 a[76:79], %1 offset:8704\n\t"
      "v_mfma_f32_16x16x32_bf16 v[96:99], v[40:43], a[28:31], v[96:99]\n\t"
      "v_mfma_f32_16x16x32_bf16 v[96:99], v[36:39], a[24:27], v[96:99]\n\t"
      "ds_read_b128 a[80:83], %1 offset:10880\n\t"
      "v_mfma_f32_16x16x32_bf16 v[92:95], v[36:39], a[20:23], v[92:95]\n\t"
      "v_mfma_f32_16x16x32_bf16 v[92:95], v[40:43], a[24:27], v[92:95]\n\t"
      "ds_read_b128 a[84:87], %1 offset:13056\n\t"
      "v_mfma_f32_16x16x32_bf16 v[88:91], v[40:43], a[20:23], v[88:91]\n\t"
      "v_mfma_f32_16x16x32_bf16 v[88:91], v[36:39], a[16:19], v[88:91]\n\t"
      "ds_read_b128 a[88:91], %1 offset:15232\n\t"
      "v_mfma_f32_16x16x32_bf16 v[84:87], v[36:39], a[12:15], v[84:87]\n\t"
      "v_mfma_f32_16x16x32_bf16 v[84:87], v[40:43], a[16:19], v[84:87]\n\t"
      "v_mfma_f32_16x16x32_bf16 v[80:83], v[40:43], a[12:15], v[80:83]\n\t"
      "v_mfma_f32_16x16x32_bf16 v[80:83], v[36:39], a[8:11], v[80:83]\n\t"
      "s_waitcnt lgkmcnt(4)\n\t"
      "v_mfma_f32_16x16x32_bf16 v[80:83], v[28:31], a[16:19], v[80:83]\n\t"
      "ds_read_b64_tr_b16 v[36:37], %0 offset:8192\n\t"
      "v_mfma_f32_16x16x32_bf16 v[80:83], v[32:35], a[20:23], v[80:83]\n\t"
      "ds_read_b64_tr_b16 v[38:39], %0 offset:8704\n\t"
      "v_mfma_f32_16x16x32_bf16 v[84:87], v[32:35], a[24:27], v[84:87]\n\t"
      "ds_read_b64_tr_b16 v[40:41], %0 offset:12288\n\t"
      "v_mfma_f32_16x16x32_bf16 v[84:87], v[28:31], a[20:23], v[84:87]\n\t"
      "ds_read_b64_tr_b16 v[42:43], %0 offset:12800\n\t"
      "v_mfma_f32_16x16x32_bf16 v[88:91], v[28:31], a[24:27], v[88:91]\n\t"
      "s_add_u32 %3, %3, 16384\n\t"
      "v_mfma_f32_16x16x32_bf16 v[88:91], v[32:35], a[28:31], v[88:91]\n\t"
      "ds_read_b128 a[92:95], %2 offset:0\n\t"
      "v_mfma_f32_16x16x32_bf16 v[92:95], v[32:35], a[32:35], v[92:95]\n\t"
      "s_cmp_eq_u32 %3, 98304\n\t"
      "v_mfma_f32_16x16x32_bf16 v[92:95], v[28:31], a[28:31], v[92:95]\n\t"
      "ds_read_b128 a[96:99], %2 offset:2176\n\t"
      "v_mfma_f32_16x16x32_bf16 v[96:99], v[28:31], a[32:35], v[96:99]\n\t"
      "s_cselect_b32 %3, 0, %3\n\t"
      "v_mfma_f32_16x16x32_bf16 v[96:99], v[32:35], a[36:39], v[96:99]\n\t"
      "ds_read_b128 a[100:103], %2 offset:4352\n\t"
      "v_mfma_f32_16x16x32_bf16 v[100:103], v[32:35], a[40:43], v[100:103]\n\t"
      "v_add_u32 %0, %3, %7\n\t"
      "v_mfma_f32_16x16x32_bf16 v[100:103], v[28:31], a[36:39], v[100:103]\n\t"
      "ds_read_b128 a[104:107], %2 offset:6528\n\t"
      "v_mfma_f32_16x16x32_bf16 v[104:107], v[28:31], a[40:43], v[104:107]\n\t"
      "v_mfma_f32_16x16x32_bf16 v[104:107], v[32:35], a[44:47], v[104:107]\n\t"
      "v_mfma_f32_16x16x32_bf16 v[108:111], v[32:35], a[48:51], v[108:111]\n\t"
      "v_mfma_f32_16x16x32_bf16 v[108:111], v[28:31], a[44:47], v[108:111]\n\t"
      "s_waitcnt vmcnt(0) lgkmcnt(4)\n\t"
      "s_barrier\n\t"
      "v_mfma_f32_16x16x32_bf16 v[108:111], v[36:39], a[52:55], v[108:111]\n\t"
      "ds_read_b64_tr_b16 v[28:29], %0 offset:0\n\t"
      "v_mfma_f32_16x16x32_bf16 v[108:111], v[40:43], a[56:59], v[108:111]\n\t"
      "ds_read_b64_tr_b16 v[30:31], %0 offset:512\n\t"
      "v_mfma_f32_16x16x32_bf16 v[104:107], v[40:43], a[52:55], v[104:107]\n\t"
      "ds_read_b64_tr_b16 v[32:33], %0 offset:4096\n\t"
      "v_mfma_f32_16x16x32_bf16 v[104:107], v[36:39], a[48:51], v[104:107]\n\t"
      "ds_read_b64_tr_b16 v[34:35], %0 offset:4608\n\t"
      "v_mfma_f32_16x16x32_bf16 v[100:103], v[36:39], a[44:47], v[100:103]\n\t"
      "v_mfma_f32_16x16x32_bf16 v[100:103], v[40:43], a[48:51], v[100:103]\n\t"
      "ds_read_b128 a[108:111], %2 offset:8704\n\t"
      "v_mfma_f32_16x16x32_bf16 v[96:99], v[40:43], a[44:47], v[96:99]\n\t"
      "v_mfma_f32_16x16x32_bf16 v[96:99], v[36:39], a[40:43], v[96:99]\n\t"
      "ds_read_b128 a[112:115], %2 offset:10880\n\t"
      "v_mfma_f32_16x16x32_bf16 v[92:95], v[36:39], a[36:39], v[92:95]\n\t"
      "v_mfma_f32_16x16x32_bf16 v[92:95], v[40:43], a[40:43], v[92:95]\n\t"
      "ds_read_b128 a[116:119], %2 offset:13056\n\t"
      "v_mfma_f32_16x16x32_bf16 v[88:91], v[40:43], a[36:39], v[88:91]\n\t"
      "v_add_u32 %1, %4, %8\n\t"
      "v_mfma_f32_16x16x32_bf16 v[88:91], v[36:39], a[32:35], v[88:91]\n\t"
      "s_cmp_eq_u32 %5, %6\n\t"
      "v_mfma_f32_16x16x32_bf16 v[84:87], v[36:39], a[28:31], v[84:87]\n\t"
      "v_mfma_f32_16x16x32_bf16 v[84:87], v[40:43], a[32:35], v[84:87]\n\t"
      "v_mfma_f32_16x16x32_bf16 v[80:83], v[40:43], a[28:31], v[80:83]\n\t"
      "v_mfma_f32_16x16x32_bf16 v[80:83], v[36:39], a[24:27], v[80:83]\n\t"
      "s_cbranch_scc1 LF_%=\n\t"
      "s_waitcnt lgkmcnt(3)\n\t"
      "v_mfma_f32_16x16x32_bf16 v[80:83], v[28:31], a[60:63], v[80:83]\n\t"
      "ds_read_b64_tr_b16 v[36:37], %0 offset:8192\n\t"
      "v_mfma_f32_16x16x32_bf16 v[80:83], v[32:35], a[64:67], v[80:83]\n\t"
      "ds_read_b64_tr_b16 v[38:39], %0 offset:8704\n\t"
      "v_mfma_f32_16x16x32_bf16 v[84:87], v[32:35], a[68:71], v[84:87]\n\t"
      "ds_read_b64_tr_b16 v[40:41], %0 offset:12288\n\t"
      "v_mfma_f32_16x16x32_bf16 v[84:87], v[28:31], a[64:67], v[84:87]\n\t"
      "ds_read_b64_tr_b16 v[42:43], %0 offset:12800\n\t"
      "v_mfma_f32_16x16x32_bf16 v[88:91], v[28:31], a[68:71], v[88:91]\n\t"
      "s_add_u32 %3, %3, 16384\n\t"
      "v_mfma_f32_16x16x32_bf16 v[88:91], v[32:35], a[72:75], v[88:91]\n\t"
      "ds_read_b128 a[0:3], %1 offset:0\n\t"
      "v_mfma_f32_16x16x32_bf16 v[92:95], v[32:35], a[76:79], v[92:95]\n\t"
      "s_cmp_eq_u32 %3, 98304\n\t"
      "v_mfma_f32_16x16x32_bf16 v[92:95], v[28:31], a[72:75], v[92:95]\n\t"
      "ds_read_b128 a[4:7], %1 offset:2176\n\t"
      "v_mfma_f32_16x16x32_bf16 v[96:99], v[28:31], a[76:79], v[96:99]\n\t"
      "s_cselect_b32 %3, 0, %3\n\t"
      "v_mfma_f32_16x16x32_bf16 v[96:99], v[32:35], a[80:83], v[96:99]\n\t"
      "ds_read_b128 a[8:11], %1 offset:4352\n\t"
      "v_mfma_f32_16x16x32_bf16 v[100:103], v[32:35], a[84:87], v[100:103]\n\t"
      "v_add_u32 %0, %3, %7\n\t"
      "v_mfma_f32_16x16x32_bf16 v[100:103], v[28:31], a[80:83], v[100:103]\n\t"
      "ds_read_b128 a[12:15], %1 offset:6528\n\t"
      "v_mfma_f32_16x16x32_bf16 v[104:107], v[28:31], a[84:87], v[104:107]\n\t"
      "v_add_u32 %2, %4, %9\n\t"
      "v_mfma_f32_16x16x32_bf16 v[104:107], v[32:35], a[88:91], v[104:107]\n\t"
      "v_mfma_f32_16x16x32_bf16 v[108:111], v[32:35], a[92:95], v[108:111]\n\t"
      "v_mfma_f32_16x16x32_bf16 v[108:111], v[28:31], a[88:91], v[108:111]\n\t"
      "s_waitcnt vmcnt(0) lgkmcnt(4)\n\t"
      "s_barrier\n\t"
      "v_mfma_f32_16x16x32_bf16 v[108:111], v[36:39], a[96:99], v[108:111]\n\t"
      "ds_read_b64_tr_b16 v[28:29], %0 offset:0\n\t"
      "v_mfma_f32_16x16x32_bf16 v[108:111], v[40:43], a[100:103], v[108:111]\n\t"
      "ds_read_b64_tr_b16 v[30:31], %0 offset:512\n\t"
      "v_mfma_f32_16x16x32_bf16 v[104:107], v[40:43], a[96:99], v[104:107]\n\t"
      "ds_read_b64_tr_b16 v[32:33], %0 offset:4096\n\t"
      "v_mfma_f32_16x16x32_bf16 v[104:107], v[36:39], a[92:95], v[104:107]\n\t"
      "ds_read_b64_tr_b16 v[34:35], %0 offset:4608\n\t"
      "v_mfma_f32_16x16x32_bf16 v[100:103], v[36:39], a[88:91], v[100:103]\n\t"
      "s_add_u32 %4, %4, 17408\n\t"
      "v_mfma_f32_16x16x32_bf16 v[100:103], v[40:43], a[92:95], v[100:103]\n\t"
      "ds_read_b128 a[16:19], %1 offset:8704\n\t"
      "v_mfma_f32_16x16x32_bf16 v[96:99], v[40:43], a[88:91], v[96:99]\n\t"
      "s_cmp_eq_u32 %4, 52224\n\t"
      "v_mfma_f32_16x16x32_bf16 v[96:99], v[36:39], a[84:87], v[96:99]\n\t"
      "ds_read_b128 a[20:23], %1 offset:10880\n\t"
      "v_mfma_f32_16x16x32_bf16 v[92:95], v[36:39], a[80:83], v[92:95]\n\t"
      "s_cselect_b32 %4, 0, %4\n\t"
      "v_mfma_f32_16x16x32_bf16 v[92:95], v[40:43], a[84:87], v[92:95]\n\t"
      "ds_read_b128 a[24:27], %1 offset:13056\n\t"
      "v_mfma_f32_16x16x32_bf16 v[88:91], v[40:43], a[80:83], v[88:91]\n\t"
      "v_mfma_f32_16x16x32_bf16 v[88:91], v[36:39], a[76:79], v[88:91]\n\t"
      "ds_read_b128 a[28:31], %1 offset:15232\n\t"
      "v_mfma_f32_16x16x32_bf16 v[84:87], v[36:39], a[72:75], v[84:87]\n\t"
      "v_mfma_f32_16x16x32_bf16 v[84:87], v[40:43], a[76:79], v[84:87]\n\t"
      "v_mfma_f32_16x16x32_bf16 v[80:83], v[40:43], a[72:75], v[80:83]\n\t"
      "v_mfma_f32_16x16x32_bf16 v[80:83], v[36:39], a[68:71], v[80:83]\n\t"
      "s_waitcnt lgkmcnt(4)\n\t"
      "v_mfma_f32_16x16x32_bf16 v[80:83], v[28:31], a[76:79], v[80:83]\n\t"
      "ds_read_b64_tr_b16 v[36:37], %0 offset:8192\n\t"
      "v_mfma_f32_16x16x32_bf16 v[80:83], v[32:35], a[80:83], v[80:83]\n\t"
      "ds_read_b64_tr_b16 v[38:39], %0 offset:8704\n\t"
      "v_mfma_f32_16x16x32_bf16 v[84:87], v[32:35], a[84:87], v[84:87]\n\t"
      "ds_read_b64_tr_b16 v[40:41], %0 offset:12288\n\t"
      "v_mfma_f32_16x16x32_bf16 v[84:87], v[28:31], a[80:83], v[84:87]\n\t"
      "ds_read_b64_tr_b16 v[42:43], %0 offset:12800\n\t"
      "v_mfma_f32_16x16x32_bf16 v[88:91], v[28:31], a[84:87], v[88:91]\n\t"
      "s_add_u32 %3, %3, 16384\n\t"
      "v_mfma_f32_16x16x32_bf16 v[88:91], v[32:35], a[88:91], v[88:91]\n\t"
      "ds_read_b128 a[32:35], %2 offset:0\n\t"
      "v_mfma_f32_16x16x32_bf16 v[92:95], v[32:35], a[92:95], v[92:95]\n\t"
      "s_cmp_eq_u32 %3, 98304\n\t"
      "v_mfma_f32_16x16x32_bf16 v[92:95], v[28:31], a[88:91], v[92:95]\n\t"
      "ds_read_b128 a[36:39], %2 offset:2176\n\t"
      "v_mfma_f32_16x16x32_bf16 v[96:99], v[28:31], a[92:95], v[96:99]\n\t"
      "s_cselect_b32 %3, 0, %3\n\t"
      "v_mfma_f32_16x16x32_bf16 v[96:99], v[32:35], a[96:99], v[96:99]\n\t"
      "ds_read_b128 a[40:43], %2 offset:4352\n\t"
      "v_mfma_f32_16x16x32_bf16 v[100:103], v[32:35], a[100:103], v[100:103]\n\t"
      "v_add_u32 %0, %3, %7\n\t"
      "v_mfma_f32_16x16x32_bf16 v[100:103], v[28:31], a[96:99], v[100:103]\n\t"
      "ds_read_b128 a[44:47], %2 offset:6528\n\t"
      "v_mfma_f32_16x16x32_bf16 v[104:107], v[28:31], a[100:103], v[104:107]\n\t"
      "v_mfma_f32_16x16x32_bf16 v[104:107], v[32:35], a[104:107], v[104:107]\n\t"
      "v_mfma_f32_16x16x32_bf16 v[108:111], v[32:35], a[108:111], v[108:111]\n\t"
      "v_mfma_f32_16x16x32_bf16 v[108:111], v[28:31], a[104:107], v[108:111]\n\t"
      "s_waitcnt vmcnt(0) lgkmcnt(4)\n\t"
      "s_barrier\n\t"
      "v_mfma_f32_16x16x32_bf16 v[108:111], v[36:39], a[112:115], v[108:111]\n\t"
      "ds_read_b64_tr_b16 v[28:29], %0 offset:0\n\t"
      "v_mfma_f32_16x16x32_bf16 v[108:111], v[40:43], a[116:119], v[108:111]\n\t"
      "ds_read_b64_tr_b16 v[30:31], %0 offset:512\n\t"
      "v_mfma_f32_16x16x32_bf16 v[104:107], v[40:43], a[112:115], v[104:107]\n\t"
      "ds_read_b64_tr_b16 v[32:33], %0 offset:4096\n\t"
      "v_mfma_f32_16x16x32_bf16 v[104:107], v[36:39], a[108:111], v[104:107]\n\t"
      "ds_read_b64_tr_b16 v[34:35], %0 offset:4608\n\t"
      "v_mfma_f32_16x16x32_bf16 v[100:103], v[36:39], a[104:107], v[100:103]\n\t"
      "v_mfma_f32_16x16x32_bf16 v[100:103], v[40:43], a[108:111], v[100:103]\n\t"
      "ds_read_b128 a[48:51], %2 offset:8704\n\t"
      "v_mfma_f32_16x16x32_bf16 v[96:99], v[40:43], a[104:107], v[96:99]\n\t"
      "v_mfma_f32_16x16x32_bf16 v[96:99], v[36:39], a[100:103], v[96:99]\n\t"
      "ds_read_b128 a[52:55], %2 offset:10880\n\t"
      "v_mfma_f32_16x16x32_bf16 v[92:95], v[36:39], a[96:99], v[92:95]\n\t"
      "v_mfma_f32_16x16x32_bf16 v[92:95], v[40:43], a[100:103], v[92:95]\n\t"
      "ds_read_b128 a[56:59], %2 offset:13056\n\t"
      "v_mfma_f32_16x16x32_bf16 v[88:91], v[40:43], a[96:99], v[88:91]\n\t"
      "v_xor_b32 %1, 64, %1\n\t"
      "v_mfma_f32_16x16x32_bf16 v[88:91], v[36:39], a[92:95], v[88:91]\n\t"
      "s_sub_u32 %5, %5, 1\n\t"
      "v_mfma_f32_16x16x32_bf16 v[84:87], v[36:39], a[88:91], v[84:87]\n\t"
      "s_cmp_lg_u32 %5, 0\n\t"
      "v_mfma_f32_16x16x32_bf16 v[84:87], v[40:43], a[92:95], v[84:87]\n\t"
      "v_mfma_f32_16x16x32_bf16 v[80:83], v[40:43], a[88:91], v[80:83]\n\t"
      "v_mfma_f32_16x16x32_bf16 v[80:83], v[36:39], a[84:87], v[80:83]\n\t"
      "s_cbranch_scc1 LQ_%=\n\t"
      "s_mov_b32 %5, 4\n\t"
      ".p2align 5\n\t"
      "LP_%=:\n\t"
      "s_waitcnt lgkmcnt(3)\n\t"
      "v_mfma_f32_16x16x32_bf16 v[48:51], v[28:31], a[0:3], v[48:51]\n\t"
      "ds_read_b64_tr_b16 v[36:37], %0 offset:8192\n\t"
      "v_mfma_f32_16x16x32_bf16 v[48:51], v[32:35], a[4:7], v[48:51]\n\t"
      "ds_read_b64_tr_b16 v[38:39], %0 offset:8704\n\t"
      "v_mfma_f32_16x16x32_bf16 v[52:55], v[32:35], a[8:11], v[52:55]\n\t"
      "ds_read_b64_tr_b16 v[40:41], %0 offset:12288\n\t"
      "v_mfma_f32_16x16x32_bf16 v[52:55], v[28:31], a[4:7], v[52:55]\n\t"
      "ds_read_b64_tr_b16 v[42:43], %0 offset:12800\n\t"
      "v_mfma_f32_16x16x32_bf16 v[56:59], v[28:31], a[8:11], v[56:59]\n\t"
      "s_add_u32 %3, %3, 16384\n\t"
      "v_mfma_f32_16x16x32_bf16 v[56:59], v[32:35], a[12:15], v[56:59]\n\t"
      "ds_read_b128 a[60:63], %1 offset:0\n\t"
      "v_mfma_f32_16x16x32_bf16 v[60:63], v[32:35], a[16:19], v[60:63]\n\t"
      "s_cmp_eq_u32 %3, 98304\n\t"
      "v_mfma_f32_16x16x32_bf16 v[60:63], v[28:31], a[12:15], v[60:63]\n\t"
      "ds_read_b128 a[64:67], %1 offset:2176\n\t"
      "v_mfma_f32_16x16x32_bf16 v[64:67], v[28:31], a[16:19], v[64:67]\n\t"
      "s_cselect_b32 %3, 0, %3\n\t"
      "v_mfma_f32_16x16x32_bf16 v[64:67], v[32:35], a[20:23], v[64:67]\n\t"
      "ds_read_b128 a[68:71], %1 offset:4352\n\t"
      "v_mfma_f32_16x16x32_bf16 v[68:71], v[32:35], a[24:27], v[68:71]\n\t"
      "v_add_u32 %0, %3, %7\n\t"
      "v_mfma_f32_16x16x32_bf16 v[68:71], v[28:31], a[20:23], v[68:71]\n\t"
      "ds_read_b128 a[72:75], %1 offset:6528\n\t"
      "v_mfma_f32_16x16x32_bf16 v[72:75], v[28:31], a[24:27], v[72:75]\n\t"
      "v_xor_b32 %2, 64, %2\n\t"
      "v_mfma_f32_16x16x32_bf16 v[72:75], v[32:35], a[28:31], v[72:75]\n\t"
      "v_mfma_f32_16x16x32_bf16 v[76:79], v[32:35], a[32:35], v[76:79]\n\t"
      "v_mfma_f32_16x16x32_bf16 v[76:79], v[28:31], a[28:31], v[76:79]\n\t"
      "s_waitcnt vmcnt(0) lgkmcnt(4)\n\t"
      "s_barrier\n\t"
      "v_mfma_f32_16x16x32_bf16 v[76:79], v[36:39], a[36:39], v[76:79]\n\t"
      "ds_read_b64_tr_b16 v[28:29], %0 offset:0\n\t"
      "v_mfma_f32_16x16x32_bf16 v[76:79], v[40:43], a[40:43], v[76:79]\n\t"
      "ds_read_b64_tr_b16 v[30:31], %0 offset:512\n\t"
      "v_mfma_f32_16x16x32_bf16 v[72:75], v[40:43], a[36:39], v[72:75]\n\t"
      "ds_read_b64_tr_b16 v[32:33], %0 offset:4096\n\t"
      "v_mfma_f32_16x16x32_bf16 v[72:75], v[36:39], a[32:35], v[72:75]\n\t"
      "ds_read_b64_tr_b16 v[34:35], %0 offset:4608\n\t"
      "v_mfma_f32_16x16x32_bf16 v[68:71], v[36:39], a[28:31], v[68:71]\n\t"
      "v_mfma_f32_16x16x32_bf16 v[68:71], v[40:43], a[32:35], v[68:71]\n\t"
      "ds_read_b128 a[76:79], %1 offset:8704\n\t"
      "v_mfma_f32_16x16x32_bf16 v[64:67], v[40:43], a[28:31], v[64:67]\n\t"
      "v_mfma_f32_16x16x32_bf16 v[64:67], v[36:39], a[24:27], v[64:67]\n\t"
      "ds_read_b128 a[80:83], %1 offset:10880\n\t"
      "v_mfma_f32_16x16x32_bf16 v[60:63], v[36:39], a[20:23], v[60:63]\n\t"
      "v_mfma_f32_16x16x32_bf16 v[60:63], v[40:43], a[24:27], v[60:63]\n\t"
      "ds_read_b128 a[84:87], %1 offset:13056\n\t"
      "v_mfma_f32_16x16x32_bf16 v[56:59], v[40:43], a[20:23], v[56:59]\n\t"
      "v_mfma_f32_16x16x32_bf16 v[56:59], v[36:39], a[16:19], v[56:59]\n\t"
      "ds_read_b128 a[88:91], %1 offset:15232\n\t"
      "v_mfma_f32_16x16x32_bf16 v[52:55], v[36:39], a[12:15], v[52:55]\n\t"
      "v_mfma_f32_16x16x32_bf16 v[52:55], v[40:43], a[16:19], v[52:55]\n\t"
      "v_mfma_f32_16x16x32_bf16 v[48:51], v[40:43], a[12:15], v[48:51]\n\t"
      "v_mfma_f32_16x16x32_bf16 v[48:51], v[36:39], a[8:11], v[48:51]\n\t"
      "s_waitcnt lgkmcnt(4)\n\t"
      "v_mfma_f32_16x16x32_bf16 v[48:51], v[28:31], a[16:19], v[48:51]\n\t"
      "ds_read_b64_tr_b16 v[36:37], %0 offset:8192\n\t"
      "v_mfma_f32_16x16x32_bf16 v[48:51], v[32:35], a[20:23], v[48:51]\n\t"
      "ds_read_b64_tr_b16 v[38:39], %0 offset:8704\n\t"
      "v_mfma_f32_16x16x32_bf16 v[52:55], v[32:35], a[24:27], v[52:55]\n\t"
      "ds_read_b64_tr_b16 v[40:41], %0 offset:12288\n\t"
      "v_mfma_f32_16x16x32_bf16 v[52:55], v[28:31], a[20:23], v[52:55]\n\t"
      "ds_read_b64_tr_b16 v[42:43], %0 offset:12800\n\t"
      "v_mfma_f32_16x16x32_bf16 v[56:59], v[28:31], a[24:27], v[56:59]\n\t"
      "s_add_u32 %3, %3, 16384\n\t"
      "v_mfma_f32_16x16x32_bf16 v[56:59], v[32:35], a[28:31], v[56:59]\n\t"
      "ds_read_b128 a[92:95], %2 offset:0\n\t"
      "v_mfma_f32_16x16x32_bf16 v[60:63], v[32:35], a[32:35], v[60:63]\n\t"
      "s_cmp_eq_u32 %3, 98304\n\t"
      "v_mfma_f32_16x16x32_bf16 v[60:63], v[28:31], a[28:31], v[60:63]\n\t"
      "ds_read_b128 a[96:99], %2 offset:2176\n\t"
      "v_mfma_f32_16x16x32_bf16 v[64:67], v[28:31], a[32:35], v[64:67]\n\t"
      "s_cselect_b32 %3, 0, %3\n\t"
      "v_mfma_f32_16x16x32_bf16 v[64:67], v[32:35], a[36:39], v[64:67]\n\t"
      "ds_read_b128 a[100:103], %2 offset:4352\n\t"
      "v_mfma_f32_16x16x32_bf16 v[68:71], v[32:35], a[40:43], v[68:71]\n\t"
      "v_add_u32 %0, %3, %7\n\t"
      "v_mfma_f32_16x16x32_bf16 v[68:71], v[28:31], a[36:39], v[68:71]\n\t"
      "ds_read_b128 a[104:107], %2 offset:6528\n\t"
      "v_mfma_f32_16x16x32_bf16 v[72:75], v[28:31], a[40:43], v[72:75]\n\t"
      "v_mfma_f32_16x16x32_bf16 v[72:75], v[32:35], a[44:47], v[72:75]\n\t"
      "v_mfma_f32_16x16x32_bf16 v[76:79], v[32:35], a[48:51], v[76:79]\n\t"
      "v_mfma_f32_16x16x32_bf16 v[76:79], v[28:31], a[44:47], v[76:79]\n\t"
      "s_waitcnt vmcnt(0) lgkmcnt(4)\n\t"
      "s_barrier\n\t"
      "v_mfma_f32_16x16x32_bf16 v[76:79], v[36:39], a[52:55], v[76:79]\n\t"
      "ds_read_b64_tr_b16 v[28:29], %0 offset:0\n\t"
      "v_mfma_f32_16x16x32_bf16 v[76:79], v[40:43], a[56:59], v[76:79]\n\t"
      "ds_read_b64_tr_b16 v[30:31], %0 offset:512\n\t"
      "v_mfma_f32_16x16x32_bf16 v[72:75], v[40:43], a[52:55], v[72:75]\n\t"
      "ds_read_b64_tr_b16 v[32:33], %0 offset:4096\n\t"
      "v_mfma_f32_16x16x32_bf16 v[72:75], v[36:39], a[48:51], v[72:75]\n\t"
      "ds_read_b64_tr_b16 v[34:35], %0 offset:4608\n\t"
      "v_mfma_f32_16x16x32_bf16 v[68:71], v[36:39], a[44:47], v[68:71]\n\t"
      "v_mfma_f32_16x16x32_bf16 v[68:71], v[40:43], a[48:51], v[68:71]\n\t"
      "ds_read_b128 a[108:111], %2 offset:8704\n\t"
      "v_mfma_f32_16x16x32_bf16 v[64:67], v[40:43], a[44:47], v[64:67]\n\t"
      "v_mfma_f32_16x16x32_bf16 v[64:67], v[36:39], a[40:43], v[64:67]\n\t"
      "ds_read_b128 a[112:115], %2 offset:10880\n\t"
      "v_mfma_f32_16x16x32_bf16 v[60:63], v[36:39], a[36:39], v[60:63]\n\t"
      "v_mfma_f32_16x16x32_bf16 v[60:63], v[40:43], a[40:43], v[60:63]\n\t"
      "ds_read_b128 a[116:119], %2 offset:13056\n\t"
      "v_mfma_f32_16x16x32_bf16 v[56:59], v[40:43], a[36:39], v[56:59]\n\t"
      "v_add_u32 %1, %4, %8\n\t"
      "v_mfma_f32_16x16x32_bf16 v[56:59], v[36:39], a[32:35], v[56:59]\n\t"
      "v_mfma_f32_16x16x32_bf16 v[52:55], v[36:39], a[28:31], v[52:55]\n\t"
      "v_mfma_f32_16x16x32_bf16 v[52:55], v[40:43], a[32:35], v[52:55]\n\t"
      "v_mfma_f32_16x16x32_bf16 v[48:51], v[40:43], a[28:31], v[48:51]\n\t"
      "v_mfma_f32_16x16x32_bf16 v[48:51], v[36:39], a[24:27], v[48:51]\n\t"
      "s_waitcnt lgkmcnt(3)\n\t"
      "v_mfma_f32_16x16x32_bf16 v[48:51], v[28:31], a[60:63], v[48:51]\n\t"
      "ds_read_b64_tr_b16 v[36:37], %0 offset:8192\n\t"
      "v_mfma_f32_16x16x32_bf16 v[48:51], v[32:35], a[64:67], v[48:51]\n\t"
      "ds_read_b64_tr_b16 v[38:39], %0 offset:8704\n\t"
      "v_mfma_f32_16x16x32_bf16 v[52:55], v[32:35], a[68:71], v[52:55]\n\t"
      "ds_read_b64_tr_b16 v[40:41], %0 offset:12288\n\t"
      "v_mfma_f32_16x16x32_bf16 v[52:55], v[28:31], a[64:67], v[52:55]\n\t"
      "ds_read_b64_tr_b16 v[42:43], %0 offset:12800\n\t"
      "v_mfma_f32_16x16x32_bf16 v[56:59], v[28:31], a[68:71], v[56:59]\n\t"
      "s_add_u32 %3, %3, 16384\n\t"
      "v_mfma_f32_16x16x32_bf16 v[56:59], v[32:35], a[72:75], v[56:59]\n\t"
      "ds_read_b128 a[0:3], %1 offset:0\n\t"
      "v_mfma_f32_16x16x32_bf16 v[60:63], v[32:35], a[76:79], v[60:63]\n\t"
      "s_cmp_eq_u32 %3, 98304\n\t"
      "v_mfma_f32_16x16x32_bf16 v[60:63], v[28:31], a[72:75], v[60:63]\n\t"
      "ds_read_b128 a[4:7], %1 offset:2176\n\t"
      "v_mfma_f32_16x16x32_bf16 v[64:67], v[28:31], a[76:79], v[64:67]\n\t"
      "s_cselect_b32 %3, 0, %3\n\t"
      "v_mfma_f32_16x16x32_bf16 v[64:67], v[32:35], a[80:83], v[64:67]\n\t"
      "ds_read_b128 a[8:11], %1 offset:4352\n\t"
      "v_mfma_f32_16x16x32_bf16 v[68:71], v[32:35], a[84:87], v[68:71]\n\t"
      "v_add_u32 %0, %3, %7\n\t"
      "v_mfma_f32_16x16x32_bf16 v[68:71], v[28:31], a[80:83], v[68:71]\n\t"
      "ds_read_b128 a[12:15], %1 offset:6528\n\t"
      "v_mfma_f32_16x16x32_bf16 v[72:75], v[28:31], a[84:87], v[72:75]\n\t"
      "v_add_u32 %2, %4, %9\n\t"
      "v_mfma_f32_16x16x32_bf16 v[72:75], v[32:35], a[88:91], v[72:75]\n\t"
      "v_mfma_f32_16x16x32_bf16 v[76:79], v[32:35], a[92:95], v[76:79]\n\t"
      "v_mfma_f32_16x16x32_bf16 v[76:79], v[28:31], a[88:91], v[76:79]\n\t"
      "s_waitcnt vmcnt(0) lgkmcnt(4)\n\t"
      "s_barrier\n\t"
      "v_mfma_f32_16x16x32_bf16 v[76:79], v[36:39], a[96:99], v[76:79]\n\t"
      "ds_read_b64_tr_b16 v[28:29], %0 offset:0\n\t"
      "v_mfma_f32_16x16x32_bf16 v[76:79], v[40:43], a[100:103], v[76:79]\n\t"
      "ds_read_b64_tr_b16 v[30:31], %0 offset:512\n\t"
      "v_mfma_f32_16x16x32_bf16 v[72:75], v[40:43], a[96:99], v[72:75]\n\t"
      "ds_read_b64_tr_b16 v[32:33], %0 offset:4096\n\t"
      "v_mfma_f32_16x16x32_bf16 v[72:75], v[36:39], a[92:95], v[72:75]\n\t"
      "ds_read_b64_tr_b16 v[34:35], %0 offset:4608\n\t"
      "v_mfma_f32_16x16x32_bf16 v[68:71], v[36:39], a[88:91], v[68:71]\n\t"
      "s_add_u32 %4, %4, 17408\n\t"
      "v_mfma_f32_16x16x32_bf16 v[68:71], v[40:43], a[92:95], v[68:71]\n\t"
      "ds_read_b128 a[16:19], %1 offset:8704\n\t"
      "v_mfma_f32_16x16x32_bf16 v[64:67], v[40:43], a[88:91], v[64:67]\n\t"
      "s_cmp_eq_u32 %4, 52224\n\t"
      "v_mfma_f32_16x16x32_bf16 v[64:67], v[36:39], a[84:87], v[64:67]\n\t"
      "ds_read_b128 a[20:23], %1 offset:10880\n\t"
      "v_mfma_f32_16x16x32_bf16 v[60:63], v[36:39], a[80:83], v[60:63]\n\t"
      "s_cselect_b32 %4, 0, %4\n\t"
      "v_mfma_f32_16x16x32_bf16 v[60:63], v[40:43], a[84:87], v[60:63]\n\t"
      "ds_read_b128 a[24:27], %1 offset:13056\n\t"
      "v_mfma_f32_16x16x32_bf16 v[56:59], v[40:43], a[80:83], v[56:59]\n\t"
      "v_mfma_f32_16x16x32_bf16 v[56:59], v[36:39], a[76:79], v[56:59]\n\t"
      "ds_read_b128 a[28:31], %1 offset:15232\n\t"
      "v_mfma_f32_16x16x32_bf16 v[52:55], v[36:39], a[72:75], v[52:55]\n\t"
      "v_mfma_f32_16x16x32_bf16 v[52:55], v[40:43], a[76:79], v[52:55]\n\t"
      "v_mfma_f32_16x16x32_bf16 v[48:51], v[40:43], a[72:75], v[48:51]\n\t"
      "v_mfma_f32_16x16x32_bf16 v[48:51], v[36:39], a[68:71], v[48:51]\n\t"
      "s_waitcnt lgkmcnt(4)\n\t"
      "v_mfma_f32_16x16x32_bf16 v[48:51], v[28:31], a[76:79], v[48:51]\n\t"
      "ds_read_b64_tr_b16 v[36:37], %0 offset:8192\n\t"
      "v_mfma_f32_16x16x32_bf16 v[48:51], v[32:35], a[80:83], v[48:51]\n\t"
      "ds_read_b64_tr_b16 v[38:39], %0 offset:8704\n\t"
      "v_mfma_f32_16x16x32_bf16 v[52:55], v[32:35], a[84:87], v[52:55]\n\t"
      "ds_read_b64_tr_b16 v[40:41], %0 offset:12288\n\t"
      "v_mfma_f32_16x16x32_bf16 v[52:55], v[28:31], a[80:83], v[52:55]\n\t"
      "ds_read_b64_tr_b16 v[42:43], %0 offset:12800\n\t"
      "v_mfma_f32_16x16x32_bf16 v[56:59], v[28:31], a[84:87], v[56:59]\n\t"
      "s_add_u32 %3, %3, 16384\n\t"
      "v_mfma_f32_16x16x32_bf16 v[56:59], v[32:35], a[88:91], v[56:59]\n\t"
      "ds_read_b128 a[32:35], %2 offset:0\n\t"
      "v_mfma_f32_16x16x32_bf16 v[60:63], v[32:35], a[92:95], v[60:63]\n\t"
      "s_cmp_eq_u32 %3, 98304\n\t"
      "v_mfma_f32_16x16x32_bf16 v[60:63], v[28:31], a[88:91], v[60:63]\n\t"
      "ds_read_b128 a[36:39], %2 offset:2176\n\t"
      "v_mfma_f32_16x16x32_bf16 v[64:67], v[28:31], a[92:95], v[64:67]\n\t"
      "s_cselect_b32 %3, 0, %3\n\t"
      "v_mfma_f32_16x16x32_bf16 v[64:67], v[32:35], a[96:99], v[64:67]\n\t"
      "ds_read_b128 a[40:43], %2 offset:4352\n\t"
      "v_mfma_f32_16x16x32_bf16 v[68:71], v[32:35], a[100:103], v[68:71]\n\t"
      "v_add_u32 %0, %3, %7\n\t"
      "v_mfma_f32_16x16x32_bf16 v[68:71], v[28:31], a[96:99], v[68:71]\n\t"
      "ds_read_b128 a[44:47], %2 offset:6528\n\t"
      "v_mfma_f32_16x16x32_bf16 v[72:75], v[28:31], a[100:103], v[72:75]\n\t"
      "v_mfma_f32_16x16x32_bf16 v[72:75], v[32:35], a[104:107], v[72:75]\n\t"
      "v_mfma_f32_16x16x32_bf16 v[76:79], v[32:35], a[108:111], v[76:79]\n\t"
      "v_mfma_f32_16x16x32_bf16 v[76:79], v[28:31], a[104:107], v[76:79]\n\t"
      "s_waitcnt vmcnt(0) lgkmcnt(4)\n\t"
      "s_barrier\n\t"
      "v_mfma_f32_16x16x32_bf16 v[76:79], v[36:39], a[112:115], v[76:79]\n\t"
      "ds_read_b64_tr_b16 v[28:29], %0 offset:0\n\t"
      "v_mfma_f32_16x16x32_bf16 v[76:79], v[40:43], a[116:119], v[76:79]\n\t"
      "ds_read_b64_tr_b16 v[30:31], %0 offset:512\n\t"
      "v_mfma_f32_16x16x32_bf16 v[72:75], v[40:43], a[112:115], v[72:75]\n\t"
      "ds_read_b64_tr_b16 v[32:33], %0 offset:4096\n\t"
      "v_mfma_f32_16x16x32_bf16 v[72:75], v[36:39], a[108:111], v[72:75]\n\t"
      "ds_read_b64_tr_b16 v[34:35], %0 offset:4608\n\t"
      "v_mfma_f32_16x16x32_bf16 v[68:71], v[36:39], a[104:107], v[68:71]\n\t"
      "v_mfma_f32_16x16x32_bf16 v[68:71], v[40:43], a[108:111], v[68:71]\n\t"
      "ds_read_b128 a[48:51], %2 offset:8704\n\t"
      "v_mfma_f32_16x16x32_bf16 v[64:67], v[40:43], a[104:107], v[64:67]\n\t"
      "v_mfma_f32_16x16x32_bf16 v[64:67], v[36:39], a[100:103], v[64:67]\n\t"
      "ds_read_b128 a[52:55], %2 offset:10880\n\t"
      "v_mfma_f32_16x16x32_bf16 v[60:63], v[36:39], a[96:99], v[60:63]\n\t"
      "v_mfma_f32_16x16x32_bf16 v[60:63], v[40:43], a[100:103], v[60:63]\n\t"
      "ds_read_b128 a[56:59], %2 offset:13056\n\t"
      "v_mfma_f32_16x16x32_bf16 v[56:59], v[40:43], a[96:99], v[56:59]\n\t"
      "v_xor_b32 %1, 64, %1\n\t"
      "v_mfma_f32_16x16x32_bf16 v[56:59], v[36:39], a[92:95], v[56:59]\n\t"
      "s_sub_u32 %5, %5, 1\n\t"
      "v_mfma_f32_16x16x32_bf16 v[52:55], v[36:39], a[88:91], v[52:55]\n\t"
      "s_cmp_lg_u32 %5, 0\n\t"
      "v_mfma_f32_16x16x32_bf16 v[52:55], v[40:43], a[92:95], v[52:55]\n\t"
      "v_mfma_f32_16x16x32_bf16 v[48:51], v[40:43], a[88:91], v[48:51]\n\t"
      "v_mfma_f32_16x16x32_bf16 v[48:51], v[36:39], a[84:87], v[48:51]\n\t"
      "s_cbranch_scc1 LP_%=\n\t"
      "s_cmp_eq_u32 %6, 1\n\t"
      "s_cbranch_scc1 LT_%=\n\t"
      "s_mov_b32 %5, 4\n\t"
      "s_mov_b32 %6, 1\n\t"
      ".p2align 5\n\t"
      "L7_%=:\n\t"
      "s_waitcnt lgkmcnt(3)\n\t"
      "v_mfma_f32_16x16x32_bf16 v[80:83], v[28:31], a[0:3], v[80:83]\n\t"
      "ds_read_b64_tr_b16 v[36:37], %0 offset:8192\n\t"
      "v_mfma_f32_16x16x32_bf16 v[80:83], v[32:35], a[4:7], v[80:83]\n\t"
      "ds_read_b64_tr_b16 v[38:39], %0 offset:8704\n\t"
      "v_mfma_f32_16x16x32_bf16 v[84:87], v[32:35], a[8:11], v[84:87]\n\t"
      "ds_read_b64_tr_b16 v[40:41], %0 offset:12288\n\t"
      "v_mfma_f32_16x16x32_bf16 v[84:87], v[28:31], a[4:7], v[84:87]\n\t"
      "ds_read_b64_tr_b16 v[42:43], %0 offset:12800\n\t"
      "v_mfma_f32_16x16x32_bf16 v[88:91], v[28:31], a[8:11], v[88:91]\n\t"
      "s_add_u32 %3, %3, 16384\n\t"
      "v_mfma_f32_16x16x32_bf16 v[88:91], v[32:35], a[12:15], v[88:91]\n\t"
      "ds_read_b128 a[60:63], %1 offset:0\n\t"
      "v_mfma_f32_16x16x32_bf16 v[92:95], v[32:35], a[16:19], v[92:95]\n\t"
      "s_cmp_eq_u32 %3, 98304\n\t"
      "v_mfma_f32_16x16x32_bf16 v[92:95], v[28:31], a[12:15], v[92:95]\n\t"
      "ds_read_b128 a[64:67], %1 offset:2176\n\t"
      "v_mfma_f32_16x16x32_bf16 v[96:99], v[28:31], a[16:19], v[96:99]\n\t"
      "s_cselect_b32 %3, 0, %3\n\t"
      "v_mfma_f32_16x16x32_bf16 v[96:99], v[32:35], a[20:23], v[96:99]\n\t"
      "ds_read_b128 a[68:71], %1 offset:4352\n\t"
      "v_mfma_f32_16x16x32_bf16 v[100:103], v[32:35], a[24:27], v[100:103]\n\t"
      "v_add_u32 %0, %3, %7\n\t"
      "v_mfma_f32_16x16x32_bf16 v[100:103], v[28:31], a[20:23], v[100:103]\n\t"
      "ds_read_b128 a[72:75], %1 offset:6528\n\t"
      "v_mfma_f32_16x16x32_bf16 v[104:107], v[28:31], a[24:27], v[104:107]\n\t"
      "v_xor_b32 %2, 64, %2\n\t"
      "v_mfma_f32_16x16x32_bf16 v[104:107], v[32:35], a[28:31], v[104:107]\n\t"
      "ds_read_b128 a[76:79], %1 offset:8704\n\t"
      "v_mfma_f32_16x16x32_bf16 v[108:111], v[32:35], a[32:35], v[108:111]\n\t"
      "v_mfma_f32_16x16x32_bf16 v[108:111], v[28:31], a[28:31], v[108:111]\n\t"
      "s_waitcnt vmcnt(0) lgkmcnt(5)\n\t"
      "s_barrier\n\t"
      "v_mfma_f32_16x16x32_bf16 v[108:111], v[36:39], a[36:39], v[108:111]\n\t"
      "ds_read_b64_tr_b16 v[28:29], %0 offset:0\n\t"
      "v_mfma_f32_16x16x32_bf16 v[108:111], v[40:43], a[40:43], v[108:111]\n\t"
      "ds_read_b64_tr_b16 v[30:31], %0 offset:512\n\t"
      "v_mfma_f32_16x16x32_bf16 v[104:107], v[40:43], a[36:39], v[104:107]\n\t"
      "ds_read_b64_tr_b16 v[32:33], %0 offset:4096\n\t"
      "v_mfma_f32_16x16x32_bf16 v[104:107], v[36:39], a[32:35], v[104:107]\n\t"
      "ds_read_b64_tr_b16 v[34:35], %0 offset:4608\n\t"
      "v_mfma_f32_16x16x32_bf16 v[100:103], v[36:39], a[28:31], v[100:103]\n\t"
      "ds_read_b64_tr_b16 v[44:45], %0 offset:8192\n\t"
      "v_mfma_f32_16x16x32_bf16 v[100:103], v[40:43], a[32:35], v[100:103]\n\t"
      "ds_read_b64_tr_b16 v[46:47], %0 offset:8704\n\t"
      "v_mfma_f32_16x16x32_bf16 v[96:99], v[40:43], a[28:31], v[96:99]\n\t"
      "s_add_u32 %3, %3, 16384\n\t"
      "v_mfma_f32_16x16x32_bf16 v[96:99], v[36:39], a[24:27], v[96:99]\n\t"
      "ds_read_b128 a[80:83], %1 offset:10880\n\t"
      "v_mfma_f32_16x16x32_bf16 v[92:95], v[36:39], a[20:23], v[92:95]\n\t"
      "s_cmp_eq_u32 %3, 98304\n\t"
      "v_mfma_f32_16x16x32_bf16 v[92:95], v[40:43], a[24:27], v[92:95]\n\t"
      "ds_read_b128 a[84:87], %1 offset:13056\n\t"
      "v_mfma_f32_16x16x32_bf16 v[88:91], v[40:43], a[20:23], v[88:91]\n\t"
      "s_cselect_b32 %3, 0, %3\n\t"
      "v_mfma_f32_16x16x32_bf16 v[88:91], v[36:39], a[16:19], v[88:91]\n\t"
      "ds_read_b128 a[88:91], %1 offset:15232\n\t"
      "v_mfma_f32_16x16x32_bf16 v[84:87], v[36:39], a[12:15], v[84:87]\n\t"
      "v_add_u32 %0, %3, %7\n\t"
      "v_mfma_f32_16x16x32_bf16 v[84:87], v[40:43], a[16:19], v[84:87]\n\t"
      "ds_read_b128 a[92:95], %2 offset:0\n\t"
      "v_mfma_f32_16x16x32_bf16 v[80:83], v[40:43], a[12:15], v[80:83]\n\t"
      "v_mfma_f32_16x16x32_bf16 v[80:83], v[36:39], a[8:11], v[80:83]\n\t"
      "s_waitcnt lgkmcnt(4)\n\t"
      "v_mfma_f32_16x16x32_bf16 v[80:83], v[28:31], a[16:19], v[80:83]\n\t"
      "v_mfma_f32_16x16x32_bf16 v[80:83], v[32:35], a[20:23], v[80:83]\n\t"
      "ds_read_b128 a[96:99], %2 offset:2176\n\t"
      "v_mfma_f32_16x16x32_bf16 v[80:83], v[44:47], a[24:27], v[80:83]\n\t"
      "v_mfma_f32_16x16x32_bf16 v[84:87], v[44:47], a[28:31], v[84:87]\n\t"
      "ds_read_b128 a[100:103], %2 offset:4352\n\t"
      "v_mfma_f32_16x16x32_bf16 v[84:87], v[32:35], a[24:27], v[84:87]\n\t"
      "v_mfma_f32_16x16x32_bf16 v[84:87], v[28:31], a[20:23], v[84:87]\n\t"
      "ds_read_b128 a[104:107], %2 offset:6528\n\t"
      "v_mfma_f32_16x16x32_bf16 v[88:91], v[28:31], a[24:27], v[88:91]\n\t"
      "v_add_u32 %1, %4, %8\n\t"
      "v_mfma_f32_16x16x32_bf16 v[88:91], v[32:35], a[28:31], v[88:91]\n\t"
      "v_mfma_f32_16x16x32_bf16 v[88:91], v[44:47], a[32:35], v[88:91]\n\t"
      "v_mfma_f32_16x16x32_bf16 v[92:95], v[44:47], a[36:39], v[92:95]\n\t"
      "s_barrier\n\t"
      "v_mfma_f32_16x16x32_bf16 v[92:95], v[32:35], a[32:35], v[92:95]\n\t"
      "ds_read_b64_tr_b16 v[36:37], %0 offset:0\n\t"
      "v_mfma_f32_16x16x32_bf16 v[92:95], v[28:31], a[28:31], v[92:95]\n\t"
      "ds_read_b64_tr_b16 v[38:39], %0 offset:512\n\t"
      "v_mfma_f32_16x16x32_bf16 v[96:99], v[28:31], a[32:35], v[96:99]\n\t"
      "ds_read_b64_tr_b16 v[40:41], %0 offset:4096\n\t"
      "v_mfma_f32_16x16x32_bf16 v[96:99], v[32:35], a[36:39], v[96:99]\n\t"
      "ds_read_b64_tr_b16 v[42:43], %0 offset:4608\n\t"
      "v_mfma_f32_16x16x32_bf16 v[96:99], v[44:47], a[40:43], v[96:99]\n\t"
      "v_mfma_f32_16x16x32_bf16 v[100:103], v[44:47], a[44:47], v[100:103]\n\t"
      "ds_read_b128 a[108:111], %2 offset:8704\n\t"
      "v_mfma_f32_16x16x32_bf16 v[100:103], v[32:35], a[40:43], v[100:103]\n\t"
      "v_mfma_f32_16x16x32_bf16 v[100:103], v[28:31], a[36:39], v[100:103]\n\t"
      "ds_read_b128 a[112:115], %2 offset:10880\n\t"
      "v_mfma_f32_16x16x32_bf16 v[104:107], v[28:31], a[40:43], v[104:107]\n\t"
      "v_mfma_f32_16x16x32_bf16 v[104:107], v[32:35], a[44:47], v[104:107]\n\t"
      "ds_read_b128 a[116:119], %2 offset:13056\n\t"
      "v_mfma_f32_16x16x32_bf16 v[104:107], v[44:47], a[48:51], v[104:107]\n\t"
      "v_mfma_f32_16x16x32_bf16 v[108:111], v[44:47], a[52:55], v[108:111]\n\t"
      "v_mfma_f32_16x16x32_bf16 v[108:111], v[32:35], a[48:51], v[108:111]\n\t"
      "v_mfma_f32_16x16x32_bf16 v[108:111], v[28:31], a[44:47], v[108:111]\n\t"
      "s_waitcnt lgkmcnt(3)\n\t"
      "v_mfma_f32_16x16x32_bf16 v[80:83], v[36:39], a[60:63], v[80:83]\n\t"
      "ds_read_b64_tr_b16 v[28:29], %0 offset:8192\n\t"
      "v_mfma_f32_16x16x32_bf16 v[80:83], v[40:43], a[64:67], v[80:83]\n\t"
      "ds_read_b64_tr_b16 v[30:31], %0 offset:8704\n\t"
      "v_mfma_f32_16x16x32_bf16 v[84:87], v[40:43], a[68:71], v[84:87]\n\t"
      "ds_read_b64_tr_b16 v[32:33], %0 offset:12288\n\t"
      "v_mfma_f32_16x16x32_bf16 v[84:87], v[36:39], a[64:67], v[84:87]\n\t"
      "ds_read_b64_tr_b16 v[34:35], %0 offset:12800\n\t"
      "v_mfma_f32_16x16x32_bf16 v[88:91], v[36:39], a[68:71], v[88:91]\n\t"
      "s_add_u32 %3, %3, 16384\n\t"
      "v_mfma_f32_16x16x32_bf16 v[88:91], v[40:43], a[72:75], v[88:91]\n\t"
      "ds_read_b128 a[0:3], %1 offset:0\n\t"
      "v_mfma_f32_16x16x32_bf16 v[92:95], v[40:43], a[76:79], v[92:95]\n\t"
      "s_cmp_eq_u32 %3, 98304\n\t"
      "v_mfma_f32_16x16x32_bf16 v[92:95], v[36:39], a[72:75], v[92:95]\n\t"
      "ds_read_b128 a[4:7], %1 offset:2176\n\t"
      "v_mfma_f32_16x16x32_bf16 v[96:99], v[36:39], a[76:79], v[96:99]\n\t"
      "s_cselect_b32 %3, 0, %3\n\t"
      "v_mfma_f32_16x16x32_bf16 v[96:99], v[40:43], a[80:83], v[96:99]\n\t"
      "ds_read_b128 a[8:11], %1 offset:4352\n\t"
      "v_mfma_f32_16x16x32_bf16 v[100:103], v[40:43], a[84:87], v[100:103]\n\t"
      "v_add_u32 %0, %3, %7\n\t"
      "v_mfma_f32_16x16x32_bf16 v[100:103], v[36:39], a[80:83], v[100:103]\n\t"
      "ds_read_b128 a[12:15], %1 offset:6528\n\t"
      "v_mfma_f32_16x16x32_bf16 v[104:107], v[36:39], a[84:87], v[104:107]\n\t"
      "v_add_u32 %2, %4, %9\n\t"
      "v_mfma_f32_16x16x32_bf16 v[104:107], v[40:43], a[88:91], v[104:107]\n\t"
      "ds_read_b128 a[16:19], %1 offset:8704\n\t"
      "v_mfma_f32_16x16x32_bf16 v[108:111], v[40:43], a[92:95], v[108:111]\n\t"
      "v_mfma_f32_16x16x32_bf16 v[108:111], v[36:39], a[88:91], v[108:111]\n\t"
      "s_waitcnt vmcnt(0) lgkmcnt(5)\n\t"
      "s_barrier\n\t"
      "v_mfma_f32_16x16x32_bf16 v[108:111], v[28:31], a[96:99], v[108:111]\n\t"
      "ds_read_b64_tr_b16 v[36:37], %0 offset:0\n\t"
      "v_mfma_f32_16x16x32_bf16 v[108:111], v[32:35], a[100:103], v[108:111]\n\t"
      "ds_read_b64_tr_b16 v[38:39], %0 offset:512\n\t"
      "v_mfma_f32_16x16x32_bf16 v[104:107], v[32:35], a[96:99], v[104:107]\n\t"
      "ds_read_b64_tr_b16 v[40:41], %0 offset:4096\n\t"
      "v_mfma_f32_16x16x32_bf16 v[104:107], v[28:31], a[92:95], v[104:107]\n\t"
      "ds_read_b64_tr_b16 v[42:43], %0 offset:4608\n\t"
      "v_mfma_f32_16x16x32_bf16 v[100:103], v[28:31], a[88:91], v[100:103]\n\t"
      "ds_read_b64_tr_b16 v[44:45], %0 offset:8192\n\t"
      "v_mfma_f32_16x16x32_bf16 v[100:103], v[32:35], a[92:95], v[100:103]\n\t"
      "ds_read_b64_tr_b16 v[46:47], %0 offset:8704\n\t"
      "v_mfma_f32_16x16x32_bf16 v[96:99], v[32:35], a[88:91], v[96:99]\n\t"
      "s_add_u32 %3, %3, 16384\n\t"
      "v_mfma_f32_16x16x32_bf16 v[96:99], v[28:31], a[84:87], v[96:99]\n\t"
      "ds_read_b128 a[20:23], %1 offset:10880\n\t"
      "v_mfma_f32_16x16x32_bf16 v[92:95], v[28:31], a[80:83], v[92:95]\n\t"
      "s_cmp_eq_u32 %3, 98304\n\t"
      "v_mfma_f32_16x16x32_bf16 v[92:95], v[32:35], a[84:87], v[92:95]\n\t"
      "ds_read_b128 a[24:27], %1 offset:13056\n\t"
      "v_mfma_f32_16x16x32_bf16 v[88:91], v[32:35], a[80:83], v[88:91]\n\t"
      "s_cselect_b32 %3, 0, %3\n\t"
      "v_mfma_f32_16x16x32_bf16 v[88:91], v[28:31], a[76:79], v[88:91]\n\t"
      "ds_read_b128 a[28:31], %1 offset:15232\n\t"
      "v_mfma_f32_16x16x32_bf16 v[84:87], v[28:31], a[72:75], v[84:87]\n\t"
      "v_add_u32 %0, %3, %7\n\t"
      "v_mfma_f32_16x16x32_bf16 v[84:87], v[32:35], a[76:79], v[84:87]\n\t"
      "ds_read_b128 a[32:35], %2 offset:0\n\t"
      "v_mfma_f32_16x16x32_bf16 v[80:83], v[32:35], a[72:75], v[80:83]\n\t"
      "v_mfma_f32_16x16x32_bf16 v[80:83], v[28:31], a[68:71], v[80:83]\n\t"
      "s_waitcnt lgkmcnt(4)\n\t"
      "v_mfma_f32_16x16x32_bf16 v[80:83], v[36:39], a[76:79], v[80:83]\n\t"
      "s_add_u32 %4, %4, 17408\n\t"
      "v_mfma_f32_16x16x32_bf16 v[80:83], v[40:43], a[80:83], v[80:83]\n\t"
      "ds_read_b128 a[36:39], %2 offset:2176\n\t"
      "v_mfma_f32_16x16x32_bf16 v[80:83], v[44:47], a[84:87], v[80:83]\n\t"
      "s_cmp_eq_u32 %4, 52224\n\t"
      "v_mfma_f32_16x16x32_bf16 v[84:87], v[44:47], a[88:91], v[84:87]\n\t"
      "ds_read_b128 a[40:43], %2 offset:4352\n\t"
      "v_mfma_f32_16x16x32_bf16 v[84:87], v[40:43], a[84:87], v[84:87]\n\t"
      "s_cselect_b32 %4, 0, %4\n\t"
      "v_mfma_f32_16x16x32_bf16 v[84:87], v[36:39], a[80:83], v[84:87]\n\t"
      "ds_read_b128 a[44:47], %2 offset:6528\n\t"
      "v_mfma_f32_16x16x32_bf16 v[88:91], v[36:39], a[84:87], v[88:91]\n\t"
      "v_xor_b32 %1, 64, %1\n\t"
      "v_mfma_f32_16x16x32_bf16 v[88:91], v[40:43], a[88:91], v[88:91]\n\t"
      "s_sub_u32 %5, %5, 1\n\t"
      "v_mfma_f32_16x16x32_bf16 v[88:91], v[44:47], a[92:95], v[88:91]\n\t"
      "s_cmp_lg_u32 %5, 0\n\t"
      "v_mfma_f32_16x16x32_bf16 v[92:95], v[44:47], a[96:99], v[92:95]\n\t"
      "s_barrier\n\t"
      "v_mfma_f32_16x16x32_bf16 v[92:95], v[40:43], a[92:95], v[92:95]\n\t"
      "ds_read_b64_tr_b16 v[28:29], %0 offset:0\n\t"
      "v_mfma_f32_16x16x32_bf16 v[92:95], v[36:39], a[88:91], v[92:95]\n\t"
      "ds_read_b64_tr_b16 v[30:31], %0 offset:512\n\t"
      "v_mfma_f32_16x16x32_bf16 v[96:99], v[36:39], a[92:95], v[96:99]\n\t"
      "ds_read_b64_tr_b16 v[32:33], %0 offset:4096\n\t"
      "v_mfma_f32_16x16x32_bf16 v[96:99], v[40:43], a[96:99], v[96:99]\n\t"
      "ds_read_b64_tr_b16 v[34:35], %0 offset:4608\n\t"
      "v_mfma_f32_16x16x32_bf16 v[96:99], v[44:47], a[100:103], v[96:99]\n\t"
      "v_mfma_f32_16x16x32_bf16 v[100:103], v[44:47], a[104:107], v[100:103]\n\t"
      "ds_read_b128 a[48:51], %2 offset:8704\n\t"
      "v_mfma_f32_16x16x32_bf16 v[100:103], v[40:43], a[100:103], v[100:103]\n\t"
      "v_mfma_f32_16x16x32_bf16 v[100:103], v[36:39], a[96:99], v[100:103]\n\t"
      "ds_read_b128 a[52:55], %2 offset:10880\n\t"
      "v_mfma_f32_16x16x32_bf16 v[104:107], v[36:39], a[100:103], v[104:107]\n\t"
      "v_mfma_f32_16x16x32_bf16 v[104:107], v[40:43], a[104:107], v[104:107]\n\t"
      "ds_read_b128 a[56:59], %2 offset:13056\n\t"
      "v_mfma_f32_16x16x32_bf16 v[104:107], v[44:47], a[108:111], v[104:107]\n\t"
      "v_mfma_f32_16x16x32_bf16 v[108:111], v[44:47], a[112:115], v[108:111]\n\t"
      "v_mfma_f32_16x16x32_bf16 v[108:111], v[40:43], a[108:111], v[108:111]\n\t"
      "v_mfma_f32_16x16x32_bf16 v[108:111], v[36:39], a[104:107], v[108:111]\n\t"
      "s_cbranch_scc1 L7_%=\n\t"
      "s_mov_b32 %5, 4\n\t"
      "s_branch LP_%=\n\t"
      "LT_%=:\n\t"
      "v_mov_b32_dpp v24, v80 row_ror:8 row_mask:0xf bank_mask:0x9\n\t"
      "v_mov_b32_dpp v25, v81 row_ror:8 row_mask:0xf bank_mask:0x9\n\t"
      "v_mov_b32_dpp v26, v82 row_ror:8 row_mask:0xf bank_mask:0x9\n\t"
      "v_mov_b32_dpp v27, v83 row_ror:8 row_mask:0xf bank_mask:0x9\n\t"
      "v_mov_b32_dpp v24, v80 row_ror:7 row_mask:0xf bank_mask:0x6\n\t"
      "v_mov_b32_dpp v25, v81 row_ror:7 row_mask:0xf bank_mask:0x6\n\t"
      "v_mov_b32_dpp v26, v82 row_ror:7 row_mask:0xf bank_mask:0x6\n\t"
      "v_mov_b32_dpp v27, v83 row_ror:7 row_mask:0xf bank_mask:0x6\n\t"
      "v_pk_add_f32 v[80:81], v[48:49], v[80:81] neg_lo:[1,1] neg_hi:[1,1]\n\t"
      "v_pk_add_f32 v[48:49], v[48:49], v[84:85]\n\t"
      "v_pk_add_f32 v[82:83], v[50:51], v[82:83] neg_lo:[1,1] neg_hi:[1,1]\n\t"
      "v_pk_add_f32 v[50:51], v[50:51], v[86:87]\n\t"
      "v_pk_add_f32 v[84:85], v[52:53], v[84:85] neg_lo:[1,1] neg_hi:[1,1]\n\t"
      "v_pk_add_f32 v[52:53], v[52:53], v[88:89]\n\t"
      "v_pk_add_f32 v[86:87], v[54:55], v[86:87] neg_lo:[1,1] neg_hi:[1,1]\n\t"
      "v_pk_add_f32 v[54:55], v[54:55], v[90:91]\n\t"
      "v_pk_add_f32 v[88:89], v[56:57], v[88:89] neg_lo:[1,1] neg_hi:[1,1]\n\t"
      "v_pk_add_f32 v[56:57], v[56:57], v[92:93]\n\t"
      "v_pk_add_f32 v[90:91], v[58:59], v[90:91] neg_lo:[1,1] neg_hi:[1,1]\n\t"
      "v_pk_add_f32 v[58:59], v[58:59], v[94:95]\n\t"
      "v_pk_add_f32 v[92:93], v[60:61], v[92:93] neg_lo:[1,1] neg_hi:[1,1]\n\t"
      "v_pk_add_f32 v[60:61], v[60:61], v[96:97]\n\t"
      "v_pk_add_f32 v[94:95], v[62:63], v[94:95] neg_lo:[1,1] neg_hi:[1,1]\n\t"
      "v_pk_add_f32 v[62:63], v[62:63], v[98:99]\n\t"
      "v_pk_add_f32 v[96:97], v[64:65], v[96:97] neg_lo:[1,1] neg_hi:[1,1]\n\t"
      "v_pk_add_f32 v[64:65], v[64:65], v[100:101]\n\t"
      "v_pk_add_f32 v[98:99], v[66:67], v[98:99] neg_lo:[1,1] neg_hi:[1,1]\n\t"
      "v_pk_add_f32 v[66:67], v[66:67], v[102:103]\n\t"
      "v_pk_add_f32 v[100:101], v[68:69], v[100:101] neg_lo:[1,1] neg_hi:[1,1]\n\t"
      "v_pk_add_f32 v[68:69], v[68:69], v[104:105]\n\t"
      "v_pk_add_f32 v[102:103], v[70:71], v[102:103] neg_lo:[1,1] neg_hi:[1,1]\n\t"
      "v_pk_add_f32 v[70:71], v[70:71], v[106:107]\n\t"
      "v_pk_add_f32 v[104:105], v[72:73], v[104:105] neg_lo:[1,1] neg_hi:[1,1]\n\t"
      "v_pk_add_f32 v[72:73], v[72:73], v[108:109]\n\t"
      "v_pk_add_f32 v[106:107], v[74:75], v[106:107] neg_lo:[1,1] neg_hi:[1,1]\n\t"
      "v_pk_add_f32 v[74:75], v[74:75], v[110:111]\n\t"
      "v_pk_add_f32 v[108:109], v[76:77], v[108:109] neg_lo:[1,1] neg_hi:[1,1]\n\t"
      "v_pk_add_f32 v[76:77], v[76:77], v[24:25]\n\t"
      "v_pk_add_f32 v[110:111], v[78:79], v[110:111] neg_lo:[1,1] neg_hi:[1,1]\n\t"
      "v_pk_add_f32 v[78:79], v[78:79], v[26:27]\n\t"
      "s_mov_b32 %5, 8\n\t"
      "s_branch LQ_%=\n\t"
      ".p2align 5\n\t"
      "LF_%=:\n\t"
      "s_waitcnt lgkmcnt(3)\n\t"
      "s_barrier\n\t"
      "v_mfma_f32_16x16x32_bf16 v[80:83], v[28:31], a[60:63], v[80:83]\n\t"
      "ds_read_b64_tr_b16 a[0:1], %0 offset:8192\n\t"
      "v_xor_b32 %1, 16, %12\n\t"
      "v_mfma_f32_16x16x32_bf16 v[80:83], v[32:35], a[64:67], v[80:83]\n\t"
      "ds_read_b64_tr_b16 a[2:3], %0 offset:8704\n\t"
      "v_mov_b32 v24, v68\n\t"
      "v_mfma_f32_16x16x32_bf16 v[84:87], v[28:31], a[64:67], v[84:87]\n\t"
      "ds_read_b64_tr_b16 a[4:5], %0 offset:12288\n\t"
      "v_mul_f32 v44, v68, v68\n\t"
      "v_mfma_f32_16x16x32_bf16 v[84:87], v[32:35], a[68:71], v[84:87]\n\t"
      "ds_read_b64_tr_b16 a[6:7], %0 offset:12800\n\t"
      "v_mov_b32 v25, v69\n\t"
      "v_mfma_f32_16x16x32_bf16 v[88:91], v[28:31], a[68:71], v[88:91]\n\t"
      "ds_read_b64_tr_b16 a[8:9], %0 offset:16384\n\t"
      "v_mul_f32 v45, v69, v69\n\t"
      "v_mfma_f32_16x16x32_bf16 v[88:91], v[32:35], a[72:75], v[88:91]\n\t"
      "ds_read_b64_tr_b16 a[10:11], %0 offset:16896\n\t"
      "v_mov_b32 v26, v70\n\t"
      "v_mfma_f32_16x16x32_bf16 v[92:95], v[28:31], a[72:75], v[92:95]\n\t"
      "ds_read_b64_tr_b16 a[12:13], %0 offset:20480\n\t"
      "v_mul_f32 v46, v70, v70\n\t"
      "v_mfma_f32_16x16x32_bf16 v[92:95], v[32:35], a[76:79], v[92:95]\n\t"
      "ds_read_b64_tr_b16 a[14:15], %0 offset:20992\n\t"
      "v_mov_b32 v27, v71\n\t"
      "v_mfma_f32_16x16x32_bf16 v[96:99], v[28:31], a[76:79], v[96:99]\n\t"
      "ds_read_b64_tr_b16 a[16:17], %0 offset:24576\n\t"
      "v_mul_f32 v47, v71, v71\n\t"
      "v_mfma_f32_16x16x32_bf16 v[96:99], v[32:35], a[80:83], v[96:99]\n\t"
      "ds_read_b64_tr_b16 a[18:19], %0 offset:25088\n\t"
      "v_add_f32 v24, v24, v72\n\t"
      "v_mfma_f32_16x16x32_bf16 v[100:103], v[28:31], a[80:83], v[100:103]\n\t"
      "ds_read_b64_tr_b16 a[20:21], %0 offset:28672\n\t"
      "v_fmac_f32 v44, v72, v72\n\t"
      "v_mfma_f32_16x16x32_bf16 v[100:103], v[32:35], a[84:87], v[100:103]\n\t"
      "ds_read_b64_tr_b16 a[22:23], %0 offset:29184\n\t"
      "v_add_f32 v25, v25, v73\n\t"
      "v_mfma_f32_16x16x32_bf16 v[104:107], v[28:31], a[84:87], v[104:107]\n\t"
      "v_fmac_f32 v45, v73, v73\n\t"
      "v_add_f32 v26, v26, v74\n\t"
      "v_mfma_f32_16x16x32_bf16 v[104:107], v[32:35], a[88:91], v[104:107]\n\t"
      "v_fmac_f32 v46, v74, v74\n\t"
      "v_add_f32 v27, v27, v75\n\t"
      "v_mfma_f32_16x16x32_bf16 v[108:111], v[28:31], a[88:91], v[108:111]\n\t"
      "v_fmac_f32 v47, v75, v75\n\t"
      "v_add_f32 v24, v24, v76\n\t"
      "v_mfma_f32_16x16x32_bf16 v[108:111], v[32:35], a[92:95], v[108:111]\n\t"
      "v_fmac_f32 v44, v76, v76\n\t"
      "v_add_f32 v25, v25, v77\n\t"
      "s_waitcnt lgkmcnt(12)\n\t"
      "ds_read_b32 %2, %10\n\t"
      "s_waitcnt lgkmcnt(11)\n\t"
      "v_mfma_f32_16x16x32_bf16 v[80:83], a[0:3], a[68:71], v[80:83]\n\t"
      "v_fmac_f32 v45, v77, v77\n\t"
      "v_add_f32 v26, v26, v78\n\t"
      "s_waitcnt lgkmcnt(9)\n\t"
      "v_mfma_f32_16x16x32_bf16 v[80:83], a[4:7], a[72:75], v[80:83]\n\t"
      "v_fmac_f32 v46, v78, v78\n\t"
      "v_add_f32 v27, v27, v79\n\t"
      "s_waitcnt lgkmcnt(7)\n\t"
      "v_mfma_f32_16x16x32_bf16 v[80:83], a[8:11], a[76:79], v[80:83]\n\t"
      "v_fmac_f32 v47, v79, v79\n\t"
      "v_add_f32 v16, v16, v48\n\t"
      "s_waitcnt lgkmcnt(5)\n\t"
      "v_mfma_f32_16x16x32_bf16 v[80:83], a[12:15], a[80:83], v[80:83]\n\t"
      "v_fmac_f32 v20, v48, v48\n\t"
      "v_add_f32 v17, v17, v49\n\t"
      "s_waitcnt lgkmcnt(3)\n\t"
      "v_mfma_f32_16x16x32_bf16 v[80:83], a[16:19], a[84:87], v[80:83]\n\t"
      "v_fmac_f32 v21, v49, v49\n\t"
      "v_add_f32 v18, v18, v50\n\t"
      "s_waitcnt lgkmcnt(1)\n\t"
      "v_mfma_f32_16x16x32_bf16 v[80:83], a[20:23], a[88:91], v[80:83]\n\t"
      "v_fmac_f32 v22, v50, v50\n\t"
      "v_add_f32 v19, v19, v51\n\t"
      "v_mfma_f32_16x16x32_bf16 v[84:87], a[0:3], a[72:75], v[84:87]\n\t"
      "v_fmac_f32 v23, v51, v51\n\t"
      "v_add_f32 v16, v16, v52\n\t"
      "v_mfma_f32_16x16x32_bf16 v[84:87], a[4:7], a[76:79], v[84:87]\n\t"
      "v_fmac_f32 v20, v52, v52\n\t"
      "v_add_f32 v17, v17, v53\n\t"
      "v_mfma_f32_16x16x32_bf16 v[84:87], a[8:11], a[80:83], v[84:87]\n\t"
      "v_add_f32 v16, v16, v80\n\t"
      "v_fmac_f32 v20, v80, v80\n\t"
      "v_mfma_f32_16x16x32_bf16 v[84:87], a[12:15], a[84:87], v[84:87]\n\t"
      "v_add_f32 v17, v17, v81\n\t"
      "v_fmac_f32 v21, v81, v81\n\t"
      "v_mfma_f32_16x16x32_bf16 v[84:87], a[16:19], a[88:91], v[84:87]\n\t"
      "v_add_f32 v18, v18, v82\n\t"
      "v_fmac_f32 v22, v82, v82\n\t"
      "v_mfma_f32_16x16x32_bf16 v[84:87], a[20:23], a[92:95], v[84:87]\n\t"
      "v_add_f32 v19, v19, v83\n\t"
      "v_fmac_f32 v23, v83, v83\n\t"
      "v_mfma_f32_16x16x32_bf16 v[88:91], a[0:3], a[76:79], v[88:91]\n\t"
      "v_fmac_f32 v21, v53, v53\n\t"
      "v_add_f32 v18, v18, v54\n\t"
      "v_mfma_f32_16x16x32_bf16 v[88:91], a[4:7], a[80:83], v[88:91]\n\t"
      "v_fmac_f32 v22, v54, v54\n\t"
      "v_add_f32 v19, v19, v55\n\t"
      "v_mfma_f32_16x16x32_bf16 v[88:91], a[8:11], a[84:87], v[88:91]\n\t"
      "v_add_f32 v16, v16, v84\n\t"
      "v_fmac_f32 v20, v84, v84\n\t"
      "v_mfma_f32_16x16x32_bf16 v[88:91], a[12:15], a[88:91], v[88:91]\n\t"
      "v_add_f32 v17, v17, v85\n\t"
      "v_fmac_f32 v21, v85, v85\n\t"
      "v_mfma_f32_16x16x32_bf16 v[88:91], a[16:19], a[92:95], v[88:91]\n\t"
      "v_add_f32 v18, v18, v86\n\t"
      "v_fmac_f32 v22, v86, v86\n\t"
      "v_mfma_f32_16x16x32_bf16 v[88:91], a[20:23], a[96:99], v[88:91]\n\t"
      "v_add_f32 v19, v19, v87\n\t"
      "v_fmac_f32 v23, v87, v87\n\t"
      "v_mfma_f32_16x16x32_bf16 v[92:95], a[0:3], a[80:83], v[92:95]\n\t"
      "v_fmac_f32 v23, v55, v55\n\t"
      "v_add_f32 v16, v16, v56\n\t"
      "v_mfma_f32_16x16x32_bf16 v[92:95], a[4:7], a[84:87], v[92:95]\n\t"
      "v_fmac_f32 v20, v56, v56\n\t"
      "v_add_f32 v17, v17, v57\n\t"
      "v_mfma_f32_16x16x32_bf16 v[92:95], a[8:11], a[88:91], v[92:95]\n\t"
      "v_add_f32 v16, v16, v88\n\t"
      "v_fmac_f32 v20, v88, v88\n\t"
      "v_mfma_f32_16x16x32_bf16 v[92:95], a[12:15], a[92:95], v[92:95]\n\t"
      "v_add_f32 v17, v17, v89\n\t"
      "v_fmac_f32 v21, v89, v89\n\t"
      "v_mfma_f32_16x16x32_bf16 v[92:95], a[16:19], a[96:99], v[92:95]\n\t"
      "v_add_f32 v18, v18, v90\n\t"
      "v_fmac_f32 v22, v90, v90\n\t"
      "v_mfma_f32_16x16x32_bf16 v[92:95], a[20:23], a[100:103], v[92:95]\n\t"
      "v_add_f32 v19, v19, v91\n\t"
      "v_fmac_f32 v23, v91, v91\n\t"
      "v_mfma_f32_16x16x32_bf16 v[96:99], a[0:3], a[84:87], v[96:99]\n\t"
      "v_fmac_f32 v21, v57, v57\n\t"
      "v_add_f32 v18, v18, v58\n\t"
      "v_mfma_f32_16x16x32_bf16 v[96:99], a[4:7], a[88:91], v[96:99]\n\t"
      "v_fmac_f32 v22, v58, v58\n\t"
      "v_add_f32 v19, v19, v59\n\t"
      "v_mfma_f32_16x16x32_bf16 v[96:99], a[8:11], a[92:95], v[96:99]\n\t"
      "v_add_f32 v16, v16, v92\n\t"
      "v_fmac_f32 v20, v92, v92\n\t"
      "v_mfma_f32_16x16x32_bf16 v[96:99], a[12:15], a[96:99], v[96:99]\n\t"
      "v_add_f32 v17, v17, v93\n\t"
      "v_fmac_f32 v21, v93, v93\n\t"
      "v_mfma_f32_16x16x32_bf16 v[96:99], a[16:19], a[100:103], v[96:99]\n\t"
      "v_add_f32 v18, v18, v94\n\t"
      "v_fmac_f32 v22, v94, v94\n\t"
      "v_mfma_f32_16x16x32_bf16 v[96:99], a[20:23], a[104:107], v[96:99]\n\t"
      "v_add_f32 v19, v19, v95\n\t"
      "v_fmac_f32 v23, v95, v95\n\t"
      "v_mfma_f32_16x16x32_bf16 v[100:103], a[0:3], a[88:91], v[100:103]\n\t"
      "v_fmac_f32 v23, v59, v59\n\t"
      "v_add_f32 v16, v16, v60\n\t"
      "v_mfma_f32_16x16x32_bf16 v[100:103], a[4:7], a[92:95], v[100:103]\n\t"
      "v_fmac_f32 v20, v60, v60\n\t"
      "v_add_f32 v17, v17, v61\n\t"
      "v_mfma_f32_16x16x32_bf16 v[100:103], a[8:11], a[96:99], v[100:103]\n\t"
      "v_add_f32 v16, v16, v96\n\t"
      "v_fmac_f32 v20, v96, v96\n\t"
      "v_mfma_f32_16x16x32_bf16 v[100:103], a[12:15], a[100:103], v[100:103]\n\t"
      "v_add_f32 v17, v17, v97\n\t"
      "v_fmac_f32 v21, v97, v97\n\t"
      "v_mfma_f32_16x16x32_bf16 v[100:103], a[16:19], a[104:107], v[100:103]\n\t"
      "v_add_f32 v18, v18, v98\n\t"
      "v_fmac_f32 v22, v98, v98\n\t"
      "v_mfma_f32_16x16x32_bf16 v[100:103], a[20:23], a[108:111], v[100:103]\n\t"
      "v_add_f32 v19, v19, v99\n\t"
      "v_fmac_f32 v23, v99, v99\n\t"
      "v_mfma_f32_16x16x32_bf16 v[104:107], a[0:3], a[92:95], v[104:107]\n\t"
      "v_fmac_f32 v21, v61, v61\n\t"
      "v_add_f32 v18, v18, v62\n\t"
      "v_mfma_f32_16x16x32_bf16 v[104:107], a[4:7], a[96:99], v[104:107]\n\t"
      "v_fmac_f32 v22, v62, v62\n\t"
      "v_add_f32 v19, v19, v63\n\t"
      "v_mfma_f32_16x16x32_bf16 v[104:107], a[8:11], a[100:103], v[104:107]\n\t"
      "v_fmac_f32 v23, v63, v63\n\t"
      "v_add_f32 v16, v16, v64\n\t"
      "v_mfma_f32_16x16x32_bf16 v[104:107], a[12:15], a[104:107], v[104:107]\n\t"
      "v_fmac_f32 v20, v64, v64\n\t"
      "v_add_f32 v17, v17, v65\n\t"
      "v_mfma_f32_16x16x32_bf16 v[104:107], a[16:19], a[108:111], v[104:107]\n\t"
      "v_fmac_f32 v21, v65, v65\n\t"
      "v_add_f32 v18, v18, v66\n\t"
      "v_mfma_f32_16x16x32_bf16 v[104:107], a[20:23], a[112:115], v[104:107]\n\t"
      "v_fmac_f32 v22, v66, v66\n\t"
      "v_add_f32 v19, v19, v67\n\t"
      "v_mfma_f32_16x16x32_bf16 v[108:111], a[0:3], a[96:99], v[108:111]\n\t"
      "v_fmac_f32 v23, v67, v67\n\t"
      "v_mfma_f32_16x16x32_bf16 v[108:111], a[4:7], a[100:103], v[108:111]\n\t"
      "ds_write_b128 %1, v[48:51] offset:256\n\t"
      "v_mfma_f32_16x16x32_bf16 v[108:111], a[8:11], a[104:107], v[108:111]\n\t"
      "ds_write_b128 %1, v[52:55] offset:768\n\t"
      "v_mfma_f32_16x16x32_bf16 v[108:111], a[12:15], a[108:111], v[108:111]\n\t"
      "v_add_f32 v24, v24, v104\n\t"
      "v_fmac_f32 v44, v104, v104\n\t"
      "v_mfma_f32_16x16x32_bf16 v[108:111], a[16:19], a[112:115], v[108:111]\n\t"
      "v_add_f32 v25, v25, v105\n\t"
      "v_fmac_f32 v45, v105, v105\n\t"
      "v_mfma_f32_16x16x32_bf16 v[108:111], a[20:23], a[116:119], v[108:111]\n\t"
      "v_add_f32 v26, v26, v106\n\t"
      "v_fmac_f32 v46, v106, v106\n\t"
      "v_add_f32 v27, v27, v107\n\t"
      "v_fmac_f32 v47, v107, v107\n\t"
      "s_andn2_b64 exec, exec, %13\n\t"
      "s_cbranch_execz LZ_%=\n\t"
      "v_mov_b32 v16, 0\n\t"
      "v_mov_b32 v17, 0\n\t"
      "v_mov_b32 v18, 0\n\t"
      "v_mov_b32 v19, 0\n\t"
      "v_mov_b32 v20, 0\n\t"
      "v_mov_b32 v21, 0\n\t"
      "v_mov_b32 v22, 0\n\t"
      "v_mov_b32 v23, 0\n\t"
      "LZ_%=:\n\t"
      "s_mov_b64 exec, %14\n\t"
      "v_add_f32 v16, v16, v100\n\t"
      "v_fmac_f32 v20, v100, v100\n\t"
      "v_add_f32 v17, v17, v101\n\t"
      "v_fmac_f32 v21, v101, v101\n\t"
      "v_add_f32 v18, v18, v102\n\t"
      "v_fmac_f32 v22, v102, v102\n\t"
      "v_add_f32 v19, v19, v103\n\t"
      "v_fmac_f32 v23, v103, v103\n\t"
      "s_mov_b64 exec, %15\n\t"
      "v_add_f32 v24, v24, v108\n\t"
      "v_fmac_f32 v44, v108, v108\n\t"
      "v_add_f32 v25, v25, v109\n\t"
      "v_fmac_f32 v45, v109, v109\n\t"
      "v_add_f32 v26, v26, v110\n\t"
      "v_fmac_f32 v46, v110, v110\n\t"
      "v_add_f32 v27, v27, v111\n\t"
      "v_fmac_f32 v47, v111, v111\n\t"
      "v_add_f32 v16, v16, v24\n\t"
      "v_add_f32 v17, v17, v25\n\t"
      "v_add_f32 v18, v18, v26\n\t"
      "v_add_f32 v19, v19, v27\n\t"
      "v_add_f32 v20, v20, v44\n\t"
      "v_add_f32 v21, v21, v45\n\t"
      "v_add_f32 v22, v22, v46\n\t"
      "v_add_f32 v23, v23, v47\n\t"
      "s_mov_b64 exec, -1\n\t"
      "s_nop 1\n\t"
      "v_add_f32_dpp v16, v16, v16 row_mirror row_mask:0xf bank_mask:0x3\n\t"
      "v_add_f32_dpp v17, v17, v17 row_mirror row_mask:0xf bank_mask:0x3\n\t"
      "v_add_f32_dpp v20, v20, v20 row_mirror row_mask:0xf bank_mask:0x3\n\t"
      "v_add_f32_dpp v21, v21, v21 row_mirror row_mask:0xf bank_mask:0x3\n\t"
      "v_add_f32_dpp v16, v18, v18 row_mirror row_mask:0xf bank_mask:0xc\n\t"
      "v_add_f32_dpp v17, v19, v19 row_mirror row_mask:0xf bank_mask:0xc\n\t"
      "v_add_f32_dpp v20, v22, v22 row_mirror row_mask:0xf bank_mask:0xc\n\t"
      "v_add_f32_dpp v21, v23, v23 row_mirror row_mask:0xf bank_mask:0xc\n\t"
      "v_add_f32_dpp v16, v16, v16 row_half_mirror row_mask:0xf bank_mask:0x5\n\t"
      "s_nop 0\n\t"
      "v_add_f32_dpp v20, v20, v20 row_half_mirror row_mask:0xf bank_mask:0x5\n\t"
      "v_add_f32_dpp v16, v17, v17 row_half_mirror row_mask:0xf bank_mask:0xa\n\t"
      "v_add_f32_dpp v20, v21, v21 row_half_mirror row_mask:0xf bank_mask:0xa\n\t"
      "s_waitcnt lgkmcnt(2)\n\t"
      "v_add_u32 v17, 1, %2\n\t"
      "v_add_f32_dpp v16, v16, v16 quad_perm:[1,0,3,2] row_mask:0xf bank_mask:0xf\n\t"
      "v_add_f32_dpp v20, v20, v20 quad_perm:[1,0,3,2] row_mask:0xf bank_mask:0xf\n\t"
      "v_add_u32 v19, 1, %2\n\t"
      "v_add_f32_dpp v16, v16, v16 quad_perm:[2,3,0,1] row_mask:0xf bank_mask:0xf\n\t"
      "v_add_f32_dpp v18, v20, v20 quad_perm:[2,3,0,1] row_mask:0xf bank_mask:0xf\n\t"
      "s_mov_b32 exec_lo, 0x11111111\n\t"
      "s_mov_b32 exec_hi, 0x11111111\n\t"
      "buffer_store_dwordx4 v[16:19], %11, %16, 0 offen sc1\n\t"
      "s_mov_b64 exec, -1\n\t"
      "s_barrier\n\t"
      "ds_write_b128 %1, v[56:59] offset:1280\n\t"
      "ds_write_b128 %1, v[60:63] offset:1792\n\t"
      "ds_write_b128 %1, v[64:67] offset:2304\n\t"
      "ds_write_b128 %1, v[68:71] offset:2816\n\t"
      "ds_write_b128 %1, v[72:75] offset:3328\n\t"
      "ds_write_b128 %1, v[76:79] offset:3840\n\t"
      "ds_write_b128 %12, v[80:83] offset:0\n\t"
      "ds_write_b128 %12, v[84:87] offset:512\n\t"
      "ds_write_b128 %12, v[88:91] offset:1024\n\t"
      "ds_write_b128 %12, v[92:95] offset:1536\n\t"
      "ds_write_b128 %12, v[96:99] offset:2048\n\t"
      "ds_write_b128 %12, v[100:103] offset:2560\n\t"
      "ds_write_b128 %12, v[104:107] offset:3072\n\t"
      "ds_write_b128 %12, v[108:111] offset:3584\n\t"
      "s_waitcnt lgkmcnt(0)\n\t"
      "s_barrier\n\t"
        : "=&v"(t0), "=&v"(t1), "=&v"(t2), "=&s"(s0), "=&s"(s1), "=&s"(s2), "=&s"(s3)
        : "v"(vW0), "v"(v00), "v"(v10), "v"(ldsb + GEN_OFF), "v"(lane * 4u), "v"(ldsb + pr * 4096u + (((4u * wv + q) ^ pr) << 4)), "s"(mA), "s"(mB), "s"(mC), "s"(srdG)
        : "memory", "scc", "a0", "a1", "a2", "a3", "a4", "a5", "a6", "a7", "a8", "a9", "a10", "a11", "a12", "a13", "a14", "a15", "a16", "a17", "a18", "a19", "a20", "a21", "a22", "a23", "a24", "a25", "a26", "a27", "a28", "a29", "a30", "a31", "a32", "a33", "a34", "a35", "a36", "a37", "a38", "a39", "a40", "a41", "a42", "a43", "a44", "a45", "a46", "a47", "a48", "a49", "a50", "a51", "a52", "a53", "a54", "a55", "a56", "a57", "a58", "a59", "a60", "a61", "a62", "a63", "a64", "a65", "a66", "a67", "a68", "a69", "a70", "a71", "a72", "a73", "a74", "a75", "a76", "a77", "a78", "a79", "a80", "a81", "a82", "a83", "a84", "a85", "a86", "a87", "a88", "a89", "a90", "a91", "a92", "a93", "a94", "a95", "a96", "a97", "a98", "a99", "a100", "a101", "a102", "a103", "a104", "a105", "a106", "a107", "a108", "a109", "a110", "a111", "a112", "a113", "a114", "a115", "a116", "a117", "a118", "a119", "v16", "v17", "v18", "v19", "v20", "v21", "v22", "v23", "v24", "v25", "v26", "v27", "v28", "v29", "v30", "v31", "v32", "v33", "v34", "v35", "v36", "v37", "v38", "v39", "v40", "v41", "v42", "v43", "v44", "v45", "v46", "v47", "v48", "v49", "v50", "v51", "v52", "v53", "v54", "v55", "v56", "v57", "v58", "v59", "v60", "v61", "v62", "v63", "v64", "v65", "v66", "v67", "v68", "v69", "v70", "v71", "v72", "v73", "v74", "v75", "v76", "v77", "v78", "v79", "v80", "v81", "v82", "v83", "v84", "v85", "v86", "v87", "v88", "v89", "v90", "v91", "v92", "v93", "v94", "v95", "v96", "v97", "v98", "v99", "v100", "v101", "v102", "v103", "v104", "v105", "v106", "v107", "v108", "v109", "v110", "v111");
      __builtin_amdgcn_s_setprio(0);
    }
    HV_READ();
  }
  LBAR();
  {
    const f32x4 a0 = *(const f32x4*)(smem + abo), a1 = *(const f32x4*)(smem + abo + 16u);
    const f32x4 b0 = *(const f32x4*)(smem + abo + 256u), b1 = *(const f32x4*)(smem + abo + 272u);
#pragma unroll
    for (int i = 0; i < 4; ++i) {
      float y[8];
#pragma unroll
      for (int c = 0; c < 4; ++c) { y[c] = gelu_tanh(__builtin_fmaf(hv[i][0][c], a0[c], b0[c])); y[4 + c] = gelu_tanh(__builtin_fmaf(hv[i][1][c], a1[c], b1[c])); }
      const u32x4 o = {cvtpk(y[0], y[1]), cvtpk(y[2], y[3]), cvtpk(y[4], y[5]), cvtpk(y[6], y[7])};
      if ((int)(32u * wv + 8u * i + rr) < nvalid) asm volatile("global_store_dwordx4 %0, %1, off sc1\n\ts_nop 1" :: "v"(op + i * 2048), "v"(o) : "memory");
    }
  }
}

extern "C" void kernel_launch(void* const* d_in, const int* in_sizes, int n_in,
                              void* d_out, int out_size, void* d_ws, size_t ws_size,
                              hipStream_t stream) {
  conv_gn_gelu<<<256, 512, 0, stream>>>((const unsigned short*)d_in[0], (const unsigned short*)d_in[1], (const unsigned short*)d_in[2],
                                        (const unsigned short*)d_in[3], (unsigned short*)d_out, (char*)d_ws);
}
```
